```python
import torch

def _softmax(x, axis=-1):
    return torch.softmax(x, dim=axis)
def _gelu(x):
    return torch.nn.functional.gelu(x, approximate="tanh")
def _silu(x):
    return torch.nn.functional.silu(x)
def _sigmoid(x):
    return torch.sigmoid(x)
def _layernorm(x, axis=-1, eps=1e-5):
    m = x.mean(dim=axis, keepdim=True)
    v = x.var(dim=axis, keepdim=True, unbiased=False)
    return (x - m) / torch.sqrt(v + eps)
def _scatter_add(tgt, idx, src, axis=0):
    return tgt.clone().index_add_(axis, idx, src)
def _segment_sum(src, seg, n):
    out = torch.zeros((n,) + src.shape[1:], dtype=src.dtype)
    return out.index_add_(0, seg, src)
def _pad(x, pw):
    flat = tuple(v for pair in reversed(pw) for v in pair)
    return torch.nn.functional.pad(x, flat)

kernel_name = 'parallel_sum__linear_attn_block__glu_gated_bilinear__prompt'

def setup_inputs():

    _w = (lambda a: (lambda t: t.to(torch.bfloat16) if t.is_floating_point() else t)(torch.as_tensor(a)))
    def _A_setup():
        B, S, D = 1, 2048, 640
        return {
            "x": _w((lambda *s: torch.randn(*s, dtype=torch.bfloat16) * 0.05)(B, S, D)),
            "wqkv": _w((lambda *s: torch.randn(*s, dtype=torch.bfloat16) * 0.05)(D, 3 * D)),
            "wo": _w((lambda *s: torch.randn(*s, dtype=torch.bfloat16) * 0.05)(D, D)),
        }
    _A_in = _A_setup()

    def _B_setup():
        B, S, D, F = 1, 2048, 640, 1536
        return {
            "x": _w((lambda *s: torch.randn(*s, dtype=torch.bfloat16) * 0.05)(B, S, D)),
            "w_gate": _w((lambda *s: torch.randn(*s, dtype=torch.bfloat16) * 0.2)(D, F) * (D ** -0.5)),
            "w_up": _w((lambda *s: torch.randn(*s, dtype=torch.bfloat16) * 0.2)(D, F) * (D ** -0.5)),
            "w_down": _w((lambda *s: torch.randn(*s, dtype=torch.bfloat16) * 0.2)(F, D) * (F ** -0.5)),
        }
    _B_in = _B_setup()

    return {
        "A_x": _A_in["x"],
        "A_wqkv": _A_in["wqkv"],
        "A_wo": _A_in["wo"],
        "B_x": _B_in["x"],
        "B_w_gate": _B_in["w_gate"],
        "B_w_up": _B_in["w_up"],
        "B_w_down": _B_in["w_down"],
    }

def reference(A_x, A_wqkv, A_wo, B_x, B_w_gate, B_w_up, B_w_down):
    def _A_ref(x, wqkv, wo):
        B, S, D = x.shape
        H = 32; Dh = D // H
        qkv = (lambda x, *d: x.permute(*d))((x @ wqkv).reshape(B, S, 3, H, Dh), 2, 0, 3, 1, 4)
        q, k, v = qkv[0], qkv[1], qkv[2]
        phi = lambda t: torch.where(t > 0, t + 1, torch.exp(t)) + 1e-6
        pq, pk = phi(q), phi(k)
        kv = torch.einsum("bhsd,bhse->bhde", pk, v)
        z = torch.einsum("bhsd->bhd", pk)
        num = torch.einsum("bhqd,bhde->bhqe", pq, kv)
        den = torch.einsum("bhqd,bhd->bhq", pq, z)[..., None] + 1e-6
        o = num / den
        return (lambda x, *d: x.permute(*d))(o, 0, 2, 1, 3).reshape(B, S, D) @ wo

    def _B_ref(x, w_gate, w_up, w_down):
        g = torch.einsum("bsd,df->bsf", x, w_gate)
        u = torch.einsum("bsd,df->bsf", x, w_up)
        h = g * u
        return torch.einsum("bsf,fd->bsd", h, w_down)

    x = A_x
    xn = _layernorm(x, axis=-1)
    return x + _A_ref(xn, A_wqkv, A_wo) + _B_ref(xn, B_w_gate, B_w_up, B_w_down)
```

```cpp
#include <hip/hip_runtime.h>
#include <stdint.h>

typedef __attribute__((ext_vector_type(8))) __bf16 bf16x8;
typedef __attribute__((ext_vector_type(4))) __bf16 bf16x4;
typedef __attribute__((ext_vector_type(2))) __bf16 bf16x2;
typedef __attribute__((ext_vector_type(16))) float f32x16;
typedef __attribute__((ext_vector_type(4))) float f32x4;
typedef __attribute__((ext_vector_type(2))) float f32x2;
typedef __attribute__((ext_vector_type(4))) unsigned u32x4;
typedef __attribute__((ext_vector_type(2))) unsigned u32x2;
typedef __amdgpu_buffer_rsrc_t rsrc_t;
typedef unsigned long long u64;
#define AS3 __attribute__((address_space(3)))
#define AS1 __attribute__((address_space(1)))
#define DEV __device__ __forceinline__
#define SB0() __builtin_amdgcn_sched_barrier(0)

constexpr uint32_t SLOT = 20480, RING = 122880, MISC = RING, LDS_BYTES = 163840;
constexpr uint32_t WS_E = 0, WS_FT = 4096, WS_FA = 8192, WS_FB = 12288, WS_FM = 16384;
constexpr uint32_t WS_KVP = 32768, KVREC = 6912;
constexpr uint32_t WS_W2 = 1u << 20, WS_A2 = 4u << 20;
constexpr uint32_t WS_USED = WS_A2 + 128u * 68u * 1024u;
constexpr uint32_t SPIN_CAP = 1u << 16;

DEV rsrc_t mk_rsrc(const void* p, uint32_t bytes) {
  return __builtin_amdgcn_make_buffer_rsrc((void*)p, 0, bytes, 0x00020000);
}
DEV void dma16(uint32_t m0v, uint32_t voff, rsrc_t srd, uint32_t soff) {
  asm volatile("s_mov_b32 m0, %0\n\ts_nop 0\n\tbuffer_load_dwordx4 %1, %2, %3 offen lds"
               :: "s"(m0v), "v"(voff), "s"(srd), "s"(soff) : "memory");
}
DEV void dma16_sc1(uint32_t m0v, uint32_t voff, rsrc_t srd, uint32_t soff) {
  asm volatile("s_mov_b32 m0, %0\n\ts_nop 0\n\tbuffer_load_dwordx4 %1, %2, %3 offen sc1 lds"
               :: "s"(m0v), "v"(voff), "s"(srd), "s"(soff) : "memory");
}
DEV void dma16_if(uint32_t cond, uint32_t m0v, uint32_t voff, rsrc_t srd, uint32_t soff) {
  asm volatile("s_cmp_eq_u32 %4, 0\n\ts_cbranch_scc1 1f\n\ts_mov_b32 m0, %0\n\ts_nop 0\n\t"
               "buffer_load_dwordx4 %1, %2, %3 offen sc1 lds\n1:"
               :: "s"(m0v), "v"(voff), "s"(srd), "s"(soff), "s"(cond) : "memory", "scc");
}
template <int N> DEV void wait_vm() { asm volatile("s_waitcnt vmcnt(%0)" :: "n"(N) : "memory"); }
DEV void bar() { asm volatile("s_waitcnt lgkmcnt(0)\n\ts_barrier" ::: "memory"); }
template <int N> DEV void open_stage() {
  asm volatile("s_waitcnt vmcnt(%0) lgkmcnt(0)\n\ts_barrier" :: "n"(N) : "memory");
}
template <int N0, int N1> DEV void open_stage2(uint32_t sel) {
  asm volatile("s_cmp_eq_u32 %2, 0\n\ts_cbranch_scc1 1f\n\ts_waitcnt vmcnt(%1)\n1:\n\ts_waitcnt vmcnt(%0) lgkmcnt(0)\n\ts_barrier"
               :: "n"(N0), "n"(N1), "s"(sel) : "memory", "scc");
}
typedef AS3 char* lptr;
DEV lptr lp(uint32_t a) { asm volatile("" : "+v"(a)); return (lptr)a; }
DEV bf16x4 trd(lptr a) { return __builtin_amdgcn_ds_read_tr16_b64_v4bf16((AS3 bf16x4*)a); }
DEV bf16x8 trd2(lptr a0, lptr a1) {
  bf16x4 lo = trd(a0), hi = trd(a1);
  return __builtin_shufflevector(lo, hi, 0, 1, 2, 3, 4, 5, 6, 7);
}
DEV u32x4 lds_r128(lptr a) { return *(AS3 u32x4*)a; }
DEV f32x4 lds_rf4(lptr a) { return *(AS3 f32x4*)a; }
DEV void lds_w128(lptr a, u32x4 v) { *(AS3 u32x4*)a = v; }
DEV float lds_rf(lptr a) { return *(AS3 float*)a; }
DEV void lds_wf(lptr a, float v) { *(AS3 float*)a = v; }
DEV int opq(int v) { asm volatile("" : "+v"(v)); return v; }
DEV uint32_t opqs(uint32_t v) { asm volatile("" : "+s"(v)); return v; }
DEV uint32_t pk2(float a, float b) {
  f32x2 v = {a, b};
  return __builtin_bit_cast(uint32_t, __builtin_convertvector(v, bf16x2));
}
DEV float blo(uint32_t u) { return __builtin_bit_cast(float, u << 16); }
DEV float bhi(uint32_t u) { return __builtin_bit_cast(float, u & 0xffff0000u); }
DEV float phi(float t) {
  float e = __builtin_amdgcn_exp2f(t * 1.4426950408889634f);
  e = __builtin_amdgcn_fmed3f(e, 0.f, 1.f);
  return fmaxf(t, 0.f) + e + 1e-6f;
}
#define MFMA32(a, b, c) __builtin_amdgcn_mfma_f32_32x32x16_bf16(a, b, c, 0, 0, 0)
#define MFMA16(a, b, c) __builtin_amdgcn_mfma_f32_16x16x32_bf16(a, b, c, 0, 0, 0)
DEV bf16x8 asbf(u32x4 v) { return __builtin_bit_cast(bf16x8, v); }
DEV u32x4 pack8(const f32x16& a, int s) {
  u32x4 r;
  r[0] = pk2(a[8 * s + 0], a[8 * s + 1]); r[1] = pk2(a[8 * s + 2], a[8 * s + 3]);
  r[2] = pk2(a[8 * s + 4], a[8 * s + 5]); r[3] = pk2(a[8 * s + 6], a[8 * s + 7]);
  return r;
}
DEV void st128_sc1(u32x4 v, rsrc_t r, uint32_t voff, uint32_t soff) {
  __builtin_amdgcn_raw_buffer_store_b128(v, r, voff, soff, 16);
}
DEV void flag_store(char* ws, uint32_t off, u64 v) {
  __hip_atomic_store((AS1 u64*)(ws + off), v, __ATOMIC_RELAXED, __HIP_MEMORY_SCOPE_AGENT);
}
DEV u64 flag_load(char* ws, uint32_t off) {
  return __hip_atomic_load((AS1 u64*)(ws + off), __ATOMIC_RELAXED, __HIP_MEMORY_SCOPE_AGENT);
}
DEV bool flag_ok(u64 v, u64 tag, uint32_t lvl) { return ((v ^ tag) >> 8) == 0 && (uint32_t)(v & 0xff) >= lvl; }
DEV u32x2 aload64(rsrc_t r, uint32_t voff, uint32_t soff) {
  u32x2 v; asm volatile("buffer_load_dwordx2 %0, %1, %2, %3 offen sc1" : "=v"(v) : "v"(voff), "s"(r), "s"(soff) : "memory"); return v;
}
DEV u32x4 aload128(rsrc_t r, uint32_t voff, uint32_t soff) {
  u32x4 v; asm volatile("buffer_load_dwordx4 %0, %1, %2, %3 offen sc1" : "=v"(v) : "v"(voff), "s"(r), "s"(soff) : "memory"); return v;
}
DEV u32x4 aload128p(rsrc_t r, uint32_t voff, uint32_t soff) {
  u32x4 v; asm volatile("buffer_load_dwordx4 %0, %1, %2, %3 offen" : "=v"(v) : "v"(voff), "s"(r), "s"(soff) : "memory"); return v;
}
template <typename T> DEV void fence_reg(T& v) { asm volatile("" : "+v"(v) :: "memory"); }
DEV u64 u64of(u32x2 v) { return ((u64)v[1] << 32) | v[0]; }
DEV void poll_wait(char* ws, uint32_t off, uint32_t lvl, u64 tag) {
  for (uint32_t it = 0; it < SPIN_CAP; ++it) {
    u64 v = flag_load(ws, off);
    if (__all(flag_ok(v, tag, lvl))) break;
    __builtin_amdgcn_s_sleep(1);
  }
}
DEV void check_or_wait(u32x2 got, char* ws, uint32_t off, uint32_t lvl, u64 tag) {
  if (!__all(flag_ok(u64of(got), tag, lvl))) poll_wait(ws, off, lvl, tag);
}
#define STAMP(i) do {} while (0)

#define NORM1(R) "v_lshlrev_b32 %1, 16, v" #R "\n\tv_and_b32 v" #R ", 0xffff0000, v" #R "\n\tv_fma_f32 %1, %1, %2, %3\n\tv_fma_f32 v" #R ", v" #R ", %2, %3\n\tv_cvt_pk_bf16_f32 v" #R ", %1, v" #R "\n\t"
#define NORMX(I, A, B, C, D) case I: asm volatile("s_cmp_eq_u32 %4, 0\n\ts_cbranch_scc1 1f\n\t" NORM1(A) NORM1(B) NORM1(C) NORM1(D) "s_nop 0\n1:" \
    : "+{v[" #A ":" #D "]}"(f), "=&v"(t) : "v"(rs), "v"(nb), "s"(cond) : "scc"); break;
DEV void norm_frag_if(uint32_t cond, int i, u32x4& f, float rs, float nb) {
  float t;
  switch (i) {
    NORMX(0, 96, 97, 98, 99) NORMX(1, 100, 101, 102, 103) NORMX(2, 104, 105, 106, 107) NORMX(3, 108, 109, 110, 111)
    NORMX(4, 112, 113, 114, 115) NORMX(5, 116, 117, 118, 119) NORMX(6, 120, 121, 122, 123) NORMX(7, 124, 125, 126, 127)
    NORMX(8, 128, 129, 130, 131) NORMX(9, 132, 133, 134, 135) NORMX(10, 136, 137, 138, 139) NORMX(11, 140, 141, 142, 143)
    NORMX(12, 144, 145, 146, 147) NORMX(13, 148, 149, 150, 151) NORMX(14, 152, 153, 154, 155) NORMX(15, 156, 157, 158, 159)
    NORMX(16, 160, 161, 162, 163) NORMX(17, 164, 165, 166, 167) NORMX(18, 168, 169, 170, 171) NORMX(19, 172, 173, 174, 175)
    NORMX(20, 176, 177, 178, 179) NORMX(21, 180, 181, 182, 183) NORMX(22, 184, 185, 186, 187) NORMX(23, 188, 189, 190, 191)
    NORMX(24, 192, 193, 194, 195) NORMX(25, 196, 197, 198, 199) NORMX(26, 200, 201, 202, 203) NORMX(27, 204, 205, 206, 207)
    NORMX(28, 208, 209, 210, 211) NORMX(29, 212, 213, 214, 215) NORMX(30, 216, 217, 218, 219) NORMX(31, 220, 221, 222, 223)
    NORMX(32, 224, 225, 226, 227) NORMX(33, 228, 229, 230, 231) NORMX(34, 232, 233, 234, 235) NORMX(35, 236, 237, 238, 239)
    NORMX(36, 240, 241, 242, 243) NORMX(37, 244, 245, 246, 247) NORMX(38, 248, 249, 250, 251) NORMX(39, 252, 253, 254, 255)
  }
}
DEV uint32_t nxt(uint32_t s) { return s + SLOT == RING ? 0u : s + SLOT; }

DEV uint32_t kvq_voff(int piece, int lane, int i, bool isq) {
  uint32_t p = piece * 1024 + lane * 16;
  uint32_t row = (p * 52429u) >> 24;
  uint32_t col = p - row * 320;
  bool hi = col >= 160;
  uint32_t c = hi ? col - 160 : col;
  return isq ? (row + (hi ? 64u : 0u)) * 3840 + 160 * i + c
             : row * 3840 + (hi ? 2560u : 1280u) + 160 * i + c;
}
DEV void check4(u32x4 ft0, u32x4 ft1, char* ws, uint32_t off, u64 tag) {
  u32x2 f0 = {ft0[0], ft0[1]}, f1 = {ft0[2], ft0[3]}, f2 = {ft1[0], ft1[1]}, f3 = {ft1[2], ft1[3]};
  bool ok = flag_ok(u64of(f0), tag, 1) && flag_ok(u64of(f1), tag, 1) && flag_ok(u64of(f2), tag, 1) && flag_ok(u64of(f3), tag, 1);
  if (!__all(ok)) {
#pragma unroll 1
    for (int j = 0; j < 4; ++j) poll_wait(ws, off + 8 * j, 1, tag);
  }
}

template <int NH>
DEV void kv_chunk(u32x4 (&xf)[40], float rs, float nb, uint32_t& cur, uint32_t& prv, int tid, int w, int ci,
                  uint32_t kv0, uint32_t kv1, uint32_t kv2, uint32_t guvo, rsrc_t rqkv, rsrc_t rg_, rsrc_t ru_) {
  const int l = tid & 63;
  const uint32_t ta = (8 * (l >> 5) + ((l >> 2) & 3)) * 320 + ((l >> 4) & 1) * 32 + 8 * (l & 3) + NH * 192;
  f32x16 a0 = {}, a1 = {}, a2 = {};
  bf16x8 w0, w1, w2, n0, n1, n2;
  norm_frag_if(1, 0, xf[0], rs, nb);
  open_stage<8>();
  lptr base = lp(cur + ta);
  w0 = trd2(base, base + 1280); w1 = trd2(base + 64, base + 64 + 1280);
  if (NH == 0) w2 = trd2(base + 128, base + 128 + 1280);
#pragma unroll
  for (int s = 0; s < 10; ++s) {
#pragma unroll
    for (int t = 0; t < 4; ++t) {
      const int ks = 4 * s + t;
      SB0();
      if (s < 5) {
        if (t == 0) dma16(prv + w * 1024, kv0, rqkv, (s + 5) * 64 * 3840);
        if (t == 1) dma16(prv + (w + 8) * 1024, kv1, rqkv, (s + 5) * 64 * 3840);
        if (t == 2 && NH == 0) dma16(prv + (w + 16) * 1024, kv2, rqkv, (s + 5) * 64 * 3840);
      } else {
        if (t == 0) dma16(prv + w * 1024, guvo, rg_, (s - 5) * 64 * 3072 + (16 + ci) * 128);
        if (t == 1) dma16(prv + (w + 8) * 1024, guvo, ru_, (s - 5) * 64 * 3072 + (16 + ci) * 128);
      }
      if (t < 3 || s < 9) {
        if (t == 3) { open_stage<8>(); prv = cur; cur = nxt(cur); base = lp(cur + ta); }
        const lptr o = base + ((t + 1) & 3) * 5120;
        n0 = trd2(o, o + 1280); n1 = trd2(o + 64, o + 64 + 1280);
        if (NH == 0) n2 = trd2(o + 128, o + 128 + 1280);
      }
      SB0();
      a0 = MFMA32(asbf(xf[ks]), w0, a0);
      a1 = MFMA32(asbf(xf[ks]), w1, a1);
      if (NH == 0) a2 = MFMA32(asbf(xf[ks]), w2, a2);
      SB0();
      if (ks < 39) norm_frag_if(1, ks + 1, xf[ks + 1], rs, nb);
      w0 = n0; w1 = n1; if (NH == 0) w2 = n2;
    }
  }
  SB0();
  const int l2 = opq(tid) & 63;
  if (NH == 0) {
#pragma unroll
    for (int j = 0; j < 16; ++j) { a0[j] = phi(a0[j]); a1[j] = phi(a1[j]); float p = phi(a2[j]); a2[j] = (l2 & 16) ? a2[j] : p; }
  }
  const lptr e0 = lp(MISC + ((NH * 12 + (w & 3)) * 2) * 1024 + l2 * 16);
  lds_w128(e0, pack8(a0, 0)); lds_w128(e0 + 1024, pack8(a0, 1));
  lds_w128(e0 + 8192, pack8(a1, 0)); lds_w128(e0 + 8192 + 1024, pack8(a1, 1));
  if (NH == 0) { lds_w128(e0 + 16384, pack8(a2, 0)); lds_w128(e0 + 16384 + 1024, pack8(a2, 1)); }
}

template <int NH>
DEV void q_chunk(u32x4 (&xf)[40], uint32_t& cur, uint32_t& prv, int tid, int w, int ci, int rg, int ct, uint32_t fb_row,
                 rsrc_t rws, char* ws, u64 tag) {
  const int l = tid & 63, rb = w & 3;
  const uint32_t ta = (8 * (l >> 5) + ((l >> 2) & 3)) * 320 + ((l >> 4) & 1) * 32 + 8 * (l & 3) + NH * 128;
  const uint32_t ZP = MISC, ZS = MISC + 5120, X3 = MISC + 5632;
  const uint32_t l16 = l * 16;
  f32x16 a0 = {}, a1 = {};
  bf16x8 w0, w1, n0, n1;
  open_stage<8>();
  const int zt = min(tid, 319), zr = (zt * 3277) >> 16;
  u32x4 zp = aload128(rws, zr * KVREC + (zt - 20 * zr) * 16, WS_KVP + ci * 16 * KVREC + 6400);
  lptr base = lp(cur + ta);
  w0 = trd2(base, base + 1280);
  if (NH == 0) w1 = trd2(base + 64, base + 64 + 1280);
#pragma unroll
  for (int s = 0; s < 5; ++s) {
#pragma unroll
    for (int t = 0; t < 8; ++t) {
      const int ks = 8 * s + t;
      SB0();
      if (t < 2 || (t == 2 && NH == 0))
        dma16_sc1(prv + (w + 8 * t) * 1024, l16, rws, WS_W2 + (ct * 340 + s * 20 + w + 8 * t) * 1024);
      if (s == 2 && t == 0) {
        if (NH == 0 && tid == 0) flag_store(ws, WS_FB + rg * 128 + ci * 8, (tag & ~0xffull) | 2);
        fence_reg(zp); lds_w128(lp(ZP + zt * 16), zp);
      }
      if (NH == 1 && s == 3 && t == 0 && w < 6) {
        const int zi = min(tid - 256, 79);
        const lptr za = lp(ZP + zi * 4);
        float z = 0.f;
#pragma unroll
        for (int j = 0; j < 16; ++j) z += lds_rf(za + j * 320);
        lds_wf(za + 5120, z);
      }
      if (t < 7 || s < 4) {
        if (t == 7) {
          if (s == 1) open_stage<4>(); else open_stage<8>();
          prv = cur; cur = nxt(cur); base = lp(cur + ta);
        }
        const int tn = (t + 1) & 7;
        const lptr o = base + (tn & 3) * 5120 + (tn >> 2) * 160;
        n0 = trd2(o, o + 1280);
        if (NH == 0) n1 = trd2(o + 64, o + 64 + 1280);
      }
      SB0();
      a0 = MFMA32(w0, asbf(xf[ks]), a0);
      if (NH == 0) a1 = MFMA32(w1, asbf(xf[ks]), a1);
      w0 = n0; if (NH == 0) w1 = n1;
    }
  }
  SB0();
  prv = cur; cur = nxt(cur);
  STAMP(8);
  const int l2 = opq(tid) & 63, r = l2 & 31, hh = l2 >> 5;
  const uint32_t pv = (r >> 4) * (68 * 1024) + (hh * 16 + (r & 15)) * 16;
  const uint32_t qb = WS_A2 + ((rg * 8 + 2 * rb) * 68 + 48) * 1024 + ci * 5 * 512;
  const lptr zs = lp(ZS + 16 * hh), x3 = lp(X3 + (32 * rb + r) * 4);
  if (NH == 0) {
    u32x2 pl = aload64(rws, (l2 & 7) * 8, fb_row);
    float gs[8];
#pragma unroll
    for (int g = 0; g < 8; ++g) {
      const f32x4 z = lds_rf4(zs + 32 * g);
      float acc = 0.f;
#pragma unroll
      for (int j = 0; j < 4; ++j) {
        float v = phi(g < 4 ? a0[4 * g + j] : a1[4 * (g - 4) + j]);
        if (g < 4) a0[4 * g + j] = v; else a1[4 * (g - 4) + j] = v;
        acc = fmaf(v, z[j], acc);
      }
      gs[g] = acc;
    }
    float p0 = gs[0] + gs[1] + (hh ? 0.f : gs[2]);
    float p1 = gs[3] + gs[4] + (hh ? gs[2] : 0.f);
    float p2 = gs[5] + gs[6] + (hh ? 0.f : gs[7]);
    if (hh) lds_wf(x3, gs[7]);
    p0 += __shfl_xor(p0, 32); p1 += __shfl_xor(p1, 32); p2 += __shfl_xor(p2, 32);
    wait_vm<0>(); fence_reg(pl);
    if (w == 0) check_or_wait(pl, ws, fb_row + (l2 & 7) * 8, 2, tag);
    bar();
    const float p3 = gs[7] + lds_rf(x3 + 512);
    const float r0 = 1.f / (p0 + 1e-6f), r1 = 1.f / (p1 + 1e-6f), r2 = 1.f / (p2 + 1e-6f), r3 = 1.f / (p3 + 1e-6f);
    const float sc[8] = {r0, r0, hh ? r1 : r0, r1, r1, r2, r2, hh ? r3 : r2};
#pragma unroll
    for (int j = 0; j < 16; ++j) { a0[j] = a0[j] * sc[j >> 2]; a1[j] = a1[j] * sc[4 + (j >> 2)]; }
    st128_sc1(pack8(a0, 0), rws, pv, qb); st128_sc1(pack8(a0, 1), rws, pv, qb + 512);
    st128_sc1(pack8(a1, 0), rws, pv, qb + 1024); st128_sc1(pack8(a1, 1), rws, pv, qb + 1536);
  } else {
    float p3 = 0.f;
#pragma unroll
    for (int g = 0; g < 2; ++g) {
      const f32x4 z = lds_rf4(zs + 256 + 32 * g);
#pragma unroll
      for (int j = 0; j < 4; ++j) { float v = phi(a0[4 * g + j]); a0[4 * g + j] = v; p3 = fmaf(v, z[j], p3); }
    }
    p3 += __shfl_xor(p3, 32);
    if (hh) lds_wf(x3 + 512, p3);
    bar();
    p3 += lds_rf(x3);
    const float r3 = 1.f / (p3 + 1e-6f);
#pragma unroll
    for (int j = 0; j < 8; ++j) a0[j] = a0[j] * r3;
    st128_sc1(pack8(a0, 0), rws, pv, qb + 2048);
  }
}

__global__ __launch_bounds__(512, 2) void fused_kernel(const void* __restrict__ gx, const void* __restrict__ gwqkv,
                                                       const void* __restrict__ gwo, const void* __restrict__ gwg,
                                                       const void* __restrict__ gwu, const void* __restrict__ gwd,
                                                       void* __restrict__ gout, char* __restrict__ ws) {
  const int tid = threadIdx.x;
  const int w = __builtin_amdgcn_readfirstlane(tid >> 6);
  const int rb = w & 3, nh = w >> 2;
  const int b = blockIdx.x;
  const int xcd = b & 7, idx = b >> 3;
  const int rg = (xcd >> 2) * 8 + (idx >> 2);
  const bool iskv = (idx & 2) != 0;
  const int ci = 2 * (xcd & 3) + (idx & 1);
  const int tt = (xcd >> 1) * 8 + (idx >> 2), ct = (xcd & 1) * 4 + (idx & 3);

  const rsrc_t rx = mk_rsrc(gx, 2048 * 1280), rqkv = mk_rsrc(gwqkv, 640 * 3840);
  const rsrc_t rg_ = mk_rsrc(gwg, 640 * 3072), ru_ = mk_rsrc(gwu, 640 * 3072);
  const rsrc_t rws = mk_rsrc(ws, WS_USED);
  STAMP(0);

  uint32_t cur = 0, prv = 5 * SLOT;
  u64 tag;
  u32x4 xf[40];
  float rs, nb;
  uint32_t guvo;
  {
    const int l = tid & 63, hh = l >> 5, r = l & 31;
    u32x2 eld = aload64(rws, 0, WS_E);
    const int t0ks = b / 5, t0cg = b - 5 * t0ks;
    if (b < 240) dma16(MISC + w * 1024, (4 * w + (l >> 4)) * 1280 + (l & 15) * 16, mk_rsrc(gwd, 1536 * 1280), t0ks * 32 * 1280 + t0cg * 256);
    const uint32_t xvo = (8 * w + (l >> 3)) * 1280 + (((l & 7) ^ ((4 * w + (l >> 4)) & 7)) << 4);
    const uint32_t xso = rg * 128 * 1280;
#pragma unroll
    for (int s = 0; s < 5; ++s) {
      dma16(s * SLOT + w * 1024, xvo, rx, xso + s * 128);
      dma16(s * SLOT + (w + 8) * 1024, xvo, rx, xso + s * 128 + 64 * 1280);
    }
    guvo = (8 * w + (l >> 3)) * 3072 + (((l & 7) ^ (((l >> 4) & 1) << 2)) << 4);
    const uint32_t xra = (32 * rb + r) * 128 + ((hh ^ ((r >> 1) & 7)) << 4);
    float s0 = 0.f, s1 = 0.f, q0 = 0.f, q1 = 0.f;
    const uint32_t kv0 = iskv ? kvq_voff(w, l, ci, false) : guvo, kv1 = iskv ? kvq_voff(w + 8, l, ci, false) : guvo;
    const uint32_t kv2 = kvq_voff((w & 3) + 16, l, ci, false);
    const rsrc_t rf0 = iskv ? rqkv : rg_, rf1 = iskv ? rqkv : ru_;
    const uint32_t fstr = iskv ? 64 * 3840 : 64 * 3072, fso = iskv ? 0 : ci * 128, third = ((idx >> 1) & 1) & (nh ^ 1);
#pragma unroll
    for (int s = 0; s < 10; ++s) {
      open_stage<8>();
#pragma unroll
      for (int t = 0; t < 4; ++t) xf[4 * s + t] = lds_r128((lptr)(cur + (xra ^ (t << 5))));
      if (s < 5) {
        dma16(prv + w * 1024, xvo, rx, xso + (s + 5) * 128);
        dma16(prv + (w + 8) * 1024, xvo, rx, xso + (s + 5) * 128 + 64 * 1280);
      } else {
        dma16(prv + w * 1024, kv0, rf0, fso + (s - 5) * fstr);
        dma16(prv + (w + 8) * 1024, kv1, rf1, fso + (s - 5) * fstr);
        dma16_if(third, prv + (w + 16) * 1024, kv2, rqkv, (s - 5) * 64 * 3840);
      }
      if (s == 0) {
        fence_reg(eld);
        u64 e = ((u64)(uint32_t)__builtin_amdgcn_readfirstlane(eld[1]) << 32) | (uint32_t)__builtin_amdgcn_readfirstlane(eld[0]);
        tag = (e * 6364136223846793005ull + 1442695040888963407ull);
        if (b < 240) {
          const int g = l >> 4;
          const lptr a = lp(MISC + (16 * (g >> 1) + 4 * (g & 1) + ((l >> 2) & 3)) * 256 + w * 32 + 8 * (l & 3));
          bf16x8 f = trd2(a, a + 8 * 256);
          const int cb = 8 * t0cg + w, c5 = cb / 5, n5 = cb - 5 * c5;
          st128_sc1(__builtin_bit_cast(u32x4, f), rws, l * 16, WS_W2 + ((c5 * 68 + t0ks) * 5 + n5) * 1024);
        }
      }
      if (s == 6 && tid == 0) flag_store(ws, WS_FT + b * 8, (tag & ~0xffull) | 1);
#pragma unroll
      for (int t = 0; t < 4; ++t) {
        const uint32_t e0 = xf[4 * s + t][0], e1 = xf[4 * s + t][1], e2 = xf[4 * s + t][2], e3 = xf[4 * s + t][3];
        asm volatile("v_dot2c_f32_bf16 %0, %8, %4\n\tv_dot2c_f32_bf16 %2, %4, %4\n\tv_dot2c_f32_bf16 %1, %8, %5\n\tv_dot2c_f32_bf16 %3, %5, %5\n\t"
                     "v_dot2c_f32_bf16 %0, %8, %6\n\tv_dot2c_f32_bf16 %2, %6, %6\n\tv_dot2c_f32_bf16 %1, %8, %7\n\tv_dot2c_f32_bf16 %3, %7, %7"
                     : "+v"(s0), "+v"(s1), "+v"(q0), "+v"(q1) : "v"(e0), "v"(e1), "v"(e2), "v"(e3), "s"(0x3f803f80u));
      }
      SB0();
      prv = cur; cur = nxt(cur);
    }
    asm volatile("s_nop 3" : "+v"(s0), "+v"(s1), "+v"(q0), "+v"(q1));
    float sm = s0 + s1, sq = q0 + q1;
    sm += __shfl_xor(sm, 32); sq += __shfl_xor(sq, 32);
    const float mean = sm * (1.f / 640.f);
    const float var = fmaxf(sq * (1.f / 640.f) - mean * mean, 0.f);
    rs = 1.0f / sqrtf(var + 1e-5f);
    nb = -mean * rs;
    STAMP(1);

    if (iskv) {
      if (nh == 0) kv_chunk<0>(xf, rs, nb, cur, prv, opq(tid), w, ci, kv0, kv1, kv2, guvo, rqkv, rg_, ru_);
      if (opqs(nh) != 0) kv_chunk<1>(xf, rs, nb, cur, prv, opq(tid), w, ci, kv0, kv1, kv2, guvo, rqkv, rg_, ru_);
      STAMP(2);
      const int t2 = opq(tid), l = t2 & 63, hh = l >> 5, r = l & 31;
      u32x4 ft0, ft1;
      if (w == 7) { ft0 = aload128(rws, l * 32, WS_FT); ft1 = aload128(rws, l * 32, WS_FT + 16); }
      bar();
      const int kb = w < 5 ? (w >> 1) : (w - 5), vb = 2 + ((w + 1) >> 1);
      const lptr ka = lp(MISC + kb * 8192 + l * 16), va = lp(MISC + vb * 8192 + l * 16);
      f32x16 d = {};
      const uint32_t ck = cur;
      if (w < 5) {
#pragma unroll
        for (int j = 0; j < 8; ++j) d = MFMA32(asbf(lds_r128(ka + j * 1024)), asbf(lds_r128(va + j * 1024)), d);
        const int ve = 32 * vb + r - 80;
        const int hv = (ve * 205) >> 12;
        const int e = ve - 20 * hv;
        const int lo = 20 * hv - 32 * kb - 4 * hh;
        const uint32_t ca = ck + (hv * 400 + e - lo * 20) * 4;
#pragma unroll
        for (int j = 0; j < 16; ++j) {
          const int rowb = (j & 3) + 8 * (j >> 2);
          if (ve >= 0 && (unsigned)(rowb - lo) < 20u) lds_wf((lptr)(ca + rowb * 80), d[j]);
        }
      } else {
        const u32x4 ones = {0x3f803f80u, 0x3f803f80u, 0x3f803f80u, 0x3f803f80u};
#pragma unroll
        for (int j = 0; j < 8; ++j) d = MFMA32(asbf(ones), asbf(lds_r128(ka + j * 1024)), d);
        if (hh == 0 && 32 * kb + r < 80) lds_wf((lptr)(ck + 6400 + (32 * kb + r) * 4), d[0]);
      }
      if (w == 7) { wait_vm<0>(); fence_reg(ft0); fence_reg(ft1); check4(ft0, ft1, ws, WS_FT + l * 32, tag); }
      bar();
      if (t2 < 420) st128_sc1(lds_r128((lptr)(ck + t2 * 16)), rws, t2 * 16, WS_KVP + (ci * 16 + rg) * KVREC);
      prv = cur; cur = nxt(cur);
      STAMP(3);
    }
  }

  {
    const int ngu = iskv ? 1 : 2;
#pragma unroll 1
    for (int it = 0; it < ngu; ++it) {
      const int l = opq(tid) & 63;
      const uint32_t ga = (8 * (l >> 5) + ((l >> 2) & 3)) * 128 + 64 * (nh ^ ((l >> 3) & 1)) + 32 * ((l >> 4) & 1) + 8 * (l & 3);
      const uint32_t first = opqs((((idx >> 1) | it) & 1) ^ 1);
      const int c = iskv ? 16 + ci : it * 8 + ci;
      uint32_t nv0, nv1, nv2, nso, nstr; rsrc_t nr0, nr1;
      if (first) { nv0 = nv1 = nv2 = guvo; nr0 = rg_; nr1 = ru_; nso = (c + 8) * 128; nstr = 64 * 3072; }
      else if (!iskv) { nv0 = kvq_voff(w, l, ci, true); nv1 = kvq_voff(w + 8, l, ci, true); nv2 = kvq_voff((w & 3) + 16, l, ci, true);
                        nr0 = nr1 = rqkv; nso = 0; nstr = 128 * 3840; }
      else { nv0 = l * 16; nv1 = l * 16 + 8192; nv2 = l * 16 + 16384; nr0 = nr1 = rws; nso = WS_W2 + (ct * 340 + w) * 1024; nstr = 20480; }
      nso = __builtin_amdgcn_readfirstlane(nso); nstr = __builtin_amdgcn_readfirstlane(nstr);
      const uint32_t third = (first | nh) ^ 1;
      const uint32_t gso = opqs(c * 128);
      f32x16 ag = {}, au = {};
      bf16x8 w0, w1, n0, n1;
      norm_frag_if(first, 0, xf[0], rs, nb);
      open_stage<8>();
      lptr base = lp(cur + ga);
      w0 = trd2(base, base + 512); w1 = trd2(base + 8192, base + 8192 + 512);
#pragma unroll
      for (int s = 0; s < 10; ++s) {
#pragma unroll
        for (int t = 0; t < 4; ++t) {
          const int ks = 4 * s + t;
          SB0();
          if (s < 5) {
            if (t == 0) dma16(prv + w * 1024, guvo, rg_, (s + 5) * 64 * 3072 + gso);
            if (t == 1) dma16(prv + (w + 8) * 1024, guvo, ru_, (s + 5) * 64 * 3072 + gso);
          } else {
            if (t == 0) dma16_sc1(prv + w * 1024, nv0, nr0, nso);
            if (t == 1) dma16_sc1(prv + (w + 8) * 1024, nv1, nr1, nso);
            if (t == 2) { dma16_if(third, prv + (w + 16) * 1024, nv2, nr1, nso); nso += nstr; }
          }
          if (s == 4 && t == 0 && iskv && tid == 0) flag_store(ws, WS_FA + (ci * 16 + rg) * 8, (tag & ~0xffull) | 1);
          if (t < 3 || s < 9) {
            if (t == 3) { open_stage<8>(); prv = cur; cur = nxt(cur); base = lp(cur + ga); }
            const lptr o = base + ((t + 1) & 3) * 2048;
            n0 = trd2(o, o + 512); n1 = trd2(o + 8192, o + 8192 + 512);
          }
          SB0();
          ag = MFMA32(w0, asbf(xf[ks]), ag);
          au = MFMA32(w1, asbf(xf[ks]), au);
          SB0();
          if (ks < 39) norm_frag_if(first, ks + 1, xf[ks + 1], rs, nb);
          w0 = n0; w1 = n1;
        }
      }
      SB0();
      prv = cur; cur = nxt(cur);
      STAMP(4 + 2 * it);
      const int l2 = opq(tid) & 63, r = l2 & 31, hh = l2 >> 5;
      const uint32_t fvo = first ? l2 * 32 : (l2 & 3) * 32, fso = first ? WS_FT : WS_FA + ci * 128;
      u32x4 ft0, ft1;
      if (w == 0) { ft0 = aload128(rws, fvo, fso); ft1 = aload128(rws, fvo, fso + 16); }
#pragma unroll
      for (int j = 0; j < 16; ++j) ag[j] = ag[j] * au[j];
      const uint32_t hv = (r >> 4) * (68 * 1024) + (hh * 16 + (r & 15)) * 16;
      const uint32_t hs = WS_A2 + ((rg * 8 + 2 * rb) * 68 + 2 * c + nh) * 1024;
      st128_sc1(pack8(ag, 0), rws, hv, hs);
      st128_sc1(pack8(ag, 1), rws, hv + 512, hs);
      if (w == 0) {
        wait_vm<2>(); fence_reg(ft0); fence_reg(ft1);
        check4(ft0, ft1, ws, fso + fvo, tag);
        if (b == 0 && first && l2 == 0) flag_store(ws, WS_E, tag);
      }
      STAMP(5 + 2 * it);
    }
  }

  uint32_t pub_off, pub_lvl;
  const uint32_t fb_row = WS_FB + (tt >> 1) * 128;
  if (!iskv) {
    if (nh == 0) q_chunk<0>(xf, cur, prv, opq(tid), w, ci, rg, ct, fb_row, rws, ws, tag);
    else q_chunk<1>(xf, cur, prv, opq(tid), w, ci, rg, ct, fb_row, rws, ws, tag);
    pub_off = WS_FB + rg * 128 + ci * 8; pub_lvl = 3;
  } else {
    const int hl = rg & 3, qd = rg >> 2, head = 4 * ci + hl;
    const int tid = opq(threadIdx.x), l = tid & 63, q4 = (l >> 2) & 3, p4 = l & 3;
    const uint32_t WO = MISC, RED = MISC + 6400;
    bar();
    u32x2 pl; u32x4 wo4, k0, k1, k2, k3;
    if (w == 0) pl = aload64(rws, (l & 7) * 8, fb_row);
    if (tid < 400) {
      const int row = (tid * 3277) >> 16, rq = (tid * 656) >> 16;
      wo4 = aload128p(mk_rsrc(gwo, 640 * 1280), row * 1280 + (tid - 20 * row) * 16, head * 20 * 1280 + qd * 320);
      const uint32_t vo = rq * 4 * KVREC + (tid - 100 * rq) * 16, so = WS_KVP + ci * 16 * KVREC + hl * 1600;
      k0 = aload128(rws, vo, so); k1 = aload128(rws, vo, so + KVREC);
      k2 = aload128(rws, vo, so + 2 * KVREC); k3 = aload128(rws, vo, so + 3 * KVREC);
    }
    wait_vm<0>();
    if (tid < 400) {
      fence_reg(wo4); fence_reg(k0); fence_reg(k1); fence_reg(k2); fence_reg(k3);
      lds_w128((lptr)(WO + tid * 16), wo4);
      f32x4 sum = (__builtin_bit_cast(f32x4, k0) + __builtin_bit_cast(f32x4, k1)) + (__builtin_bit_cast(f32x4, k2) + __builtin_bit_cast(f32x4, k3));
      lds_w128((lptr)(RED + tid * 16), __builtin_bit_cast(u32x4, sum));
    }
    if (w == 0) { fence_reg(pl); check_or_wait(pl, ws, fb_row + (l & 7) * 8, 2, tag); }
    bar();
    if (tid == 0) flag_store(ws, WS_FB + rg * 128 + (8 + ci) * 8, (tag & ~0xffull) | 1);
    STAMP(8);
    {
      const int i16 = l & 15, g = l >> 4;
      u32x4 af[2];
#pragma unroll
      for (int db = 0; db < 2; ++db) {
        const int d = min(16 * db + i16, 19);
        f32x4 lo = {}, hi = {};
        const lptr al = lp(RED + d * 80 + min(g, 2) * 32), ah = lp(RED + d * 80 + min(g, 2) * 32 + (g < 2 ? 16 : 0));
#pragma unroll
        for (int j = 0; j < 4; ++j) {
          lo += lds_rf4(al + j * 1600);
          hi += lds_rf4(ah + j * 1600);
        }
        af[db][0] = g < 3 ? pk2(lo[0], lo[1]) : 0u; af[db][1] = g < 3 ? pk2(lo[2], lo[3]) : 0u;
        af[db][2] = g < 2 ? pk2(hi[0], hi[1]) : 0u; af[db][3] = g < 2 ? pk2(hi[2], hi[3]) : 0u;
      }
      const uint32_t b0 = WO + min(8 * g + q4, 16 + q4) * 320 + 8 * p4, b1 = WO + min(8 * g + 4 + q4, 16 + q4) * 320 + 8 * p4;
      for (int n = w; n < 10; n += 8) {
        const bf16x8 bf = trd2((lptr)(b0 + n * 32), (lptr)(b1 + n * 32));
        const f32x4 z4 = {};
        const f32x4 m0 = MFMA16(asbf(af[0]), bf, z4), m1 = MFMA16(asbf(af[1]), bf, z4);
        const int cb = 10 * qd + n, c5 = cb / 5, n5 = cb - 5 * c5;
        const uint32_t fb = WS_W2 + (c5 * 340 + 240 + n5) * 1024 + i16 * 16;
        const int G0 = 5 * head + g, G1 = 5 * head + 4;
        const uint32_t o0 = fb + (G0 >> 3) * 5120 + (((G0 >> 2) & 1) * 2 + (G0 & 1)) * 256 + ((G0 >> 1) & 1) * 8;
        const uint32_t o1 = fb + (G1 >> 3) * 5120 + (((G1 >> 2) & 1) * 2 + (G1 & 1)) * 256 + ((G1 >> 1) & 1) * 8;
        u32x2 v0 = {pk2(m0[0], m0[1]), pk2(m0[2], m0[3])}, v1 = {pk2(m1[0], m1[1]), pk2(m1[2], m1[3])};
        __builtin_amdgcn_raw_buffer_store_b64(v0, rws, o0, 0, 16);
        if (g == 0) __builtin_amdgcn_raw_buffer_store_b64(v1, rws, o1, 0, 16);
      }
    }
    pub_off = WS_FM + (qd * 32 + head) * 8; pub_lvl = 1;
  }
  STAMP(9);

  {
    const int kh = nh;
    const int tid = opq(threadIdx.x), l = tid & 63;
    u32x4 A[34];
    const int pr0 = (tid * 6554) >> 16;
    const int pr1 = ((tid + 512) * 6554) >> 16;
    const uint32_t xo0 = pr0 * 1280 + (tid - 10 * pr0) * 16, xo1 = pr1 * 1280 + (tid + 512 - 10 * pr1) * 16;
    const uint32_t xs = tt * 64 * 1280 + ct * 160;
    u32x4 xr0 = aload128p(rx, xo0, xs), xr1;
    if (tid < 128) xr1 = aload128p(rx, xo1, xs);
    const uint32_t as = WS_A2 + ((tt * 4 + rb) * 68 + 2 * kh) * 1024;
    const uint32_t l16 = l * 16;
#pragma unroll
    for (int p = 0; p < 5; ++p) { A[2 * p] = aload128(rws, l16, as + p * 4096); A[2 * p + 1] = aload128(rws, l16, as + p * 4096 + 1024); }
    u32x2 pl;
    const uint32_t po2 = fb_row + 64 + (l & 7) * 8;
    const uint32_t po3 = l < 32 ? WS_FM + (ct >> 1) * 256 + l * 8 : fb_row + (l & 7) * 8;
    if (w == 0) pl = aload64(rws, po2, 0);
    f32x4 acc[5] = {};
#pragma unroll
    for (int p = 0; p < 17; ++p) {
      const int lo = p < 5 ? 0 : p - 4, hi = p - 1 < 11 ? p - 1 : 11;
      const int nop = hi >= lo ? hi - lo + 1 : 0, ex = p < 5 ? 2 * (4 - p) : 0;
      if (p == 3 || p == 7) {
        if (w == 0) {
          wait_vm<15>(); fence_reg(pl);
          if (p == 3) check_or_wait(pl, ws, po2, 1, tag); else check_or_wait(pl, ws, po3, l < 32 ? 1 : 3, tag);
        }
      }
      switch (nop * 16 + ex) {
#define OPEN_CASE(NOP, EX) case NOP * 16 + EX: open_stage2<NOP * 5 + EX, NOP * 4 + EX>(nh); break;
        OPEN_CASE(0, 8) OPEN_CASE(1, 6) OPEN_CASE(2, 4) OPEN_CASE(3, 2) OPEN_CASE(4, 0)
        OPEN_CASE(3, 0) OPEN_CASE(2, 0) OPEN_CASE(1, 0) OPEN_CASE(0, 0)
        default: open_stage<0>();
      }
      fence_reg(A[2 * p]); fence_reg(A[2 * p + 1]);
      if (p == 0 && tid == 0) flag_store(ws, pub_off, (tag & ~0xffull) | pub_lvl);
      const lptr rbs = lp(cur + kh * 10240 + l16);
      u32x4 B[10];
#pragma unroll
      for (int j = 0; j < 10; ++j) B[j] = lds_r128(rbs + j * 1024);
      SB0();
      if (p + 5 < 17) {
        const uint32_t so = WS_W2 + (ct * 340 + (p + 5) * 20 + w) * 1024;
        dma16_sc1(prv + w * 1024, l16, rws, so);
        dma16_sc1(prv + (w + 8) * 1024, l16, rws, so + 8192);
        dma16_if(nh ^ 1, prv + (w + 16) * 1024, l16, rws, so + 16384);
        A[2 * p + 10] = aload128(rws, l16, as + (p + 5) * 4096);
        A[2 * p + 11] = aload128(rws, l16, as + (p + 5) * 4096 + 1024);
      }
      if (p == 3 && w == 0) pl = aload64(rws, po3, 0);
      SB0();
#pragma unroll
      for (int j = 0; j < 10; ++j) acc[j % 5] = MFMA16(asbf(B[j]), asbf(A[2 * p + j / 5]), acc[j % 5]);
      SB0();
      prv = cur; cur = nxt(cur);
    }
    STAMP(10);
    bar();
    const lptr ow = lp((kh * 64 + rb * 16 + (l & 15)) * 336 + (l >> 4) * 16);
#pragma unroll
    for (int j = 0; j < 5; ++j) lds_w128(ow + j * 64, __builtin_bit_cast(u32x4, acc[j]));
    bar();
    const rsrc_t ro = mk_rsrc(gout, 2048 * 1280);
#pragma unroll
    for (int h = 0; h < 2; ++h) {
      if (h == 0 || tid < 128) {
        u32x4 xr = h ? xr1 : xr0;
        fence_reg(xr);
        const int pr = h ? pr1 : pr0, pc = tid + 512 * h - 10 * pr;
        const lptr a = lp(pr * 336 + pc * 32);
        f32x4 u0 = lds_rf4(a) + lds_rf4(a + 64 * 336);
        f32x4 u1 = lds_rf4(a + 16) + lds_rf4(a + 16 + 64 * 336);
        u32x4 o;
        o[0] = pk2(u0[0] + blo(xr[0]), u0[1] + bhi(xr[0])); o[1] = pk2(u0[2] + blo(xr[1]), u0[3] + bhi(xr[1]));
        o[2] = pk2(u1[0] + blo(xr[2]), u1[1] + bhi(xr[2])); o[3] = pk2(u1[2] + blo(xr[3]), u1[3] + bhi(xr[3]));
        __builtin_amdgcn_raw_buffer_store_b128(o, ro, h ? xo1 : xo0, xs, 0);
      }
    }
    STAMP(11);
  }
}

extern "C" void kernel_launch(void* const* d_in, const int* in_sizes, int n_in, void* d_out, int out_size, void* d_ws,
                              size_t ws_size, hipStream_t stream) {
  (void)in_sizes; (void)n_in; (void)out_size; (void)ws_size;
  (void)hipFuncSetAttribute((const void*)fused_kernel, hipFuncAttributeMaxDynamicSharedMemorySize, LDS_BYTES);
  fused_kernel<<<256, 512, LDS_BYTES, stream>>>(d_in[0], d_in[1], d_in[2], d_in[4], d_in[5], d_in[6], d_out, (char*)d_ws);
}
```

```cpp
#define DBG_STAMP
#include <hip/hip_runtime.h>
#include <stdint.h>

typedef __attribute__((ext_vector_type(8))) __bf16 bf16x8;
typedef __attribute__((ext_vector_type(4))) __bf16 bf16x4;
typedef __attribute__((ext_vector_type(2))) __bf16 bf16x2;
typedef __attribute__((ext_vector_type(16))) float f32x16;
typedef __attribute__((ext_vector_type(4))) float f32x4;
typedef __attribute__((ext_vector_type(2))) float f32x2;
typedef __attribute__((ext_vector_type(4))) unsigned u32x4;
typedef __attribute__((ext_vector_type(2))) unsigned u32x2;
typedef __amdgpu_buffer_rsrc_t rsrc_t;
typedef unsigned long long u64;
#define AS3 __attribute__((address_space(3)))
#define AS1 __attribute__((address_space(1)))
#define DEV __device__ __forceinline__
#define SB0() __builtin_amdgcn_sched_barrier(0)

constexpr uint32_t SLOT = 20480, RING = 122880, MISC = RING, LDS_BYTES = 163840;
constexpr uint32_t WS_E = 0, WS_FT = 4096, WS_FA = 8192, WS_FB = 12288, WS_FM = 16384;
constexpr uint32_t WS_KVP = 32768, KVREC = 6912;
constexpr uint32_t WS_W2 = 1u << 20, WS_A2 = 4u << 20;
constexpr uint32_t WS_USED = WS_A2 + 128u * 68u * 1024u;
constexpr uint32_t SPIN_CAP = 1u << 16;

DEV rsrc_t mk_rsrc(const void* p, uint32_t bytes) {
  return __builtin_amdgcn_make_buffer_rsrc((void*)p, 0, bytes, 0x00020000);
}
DEV void dma16(uint32_t m0v, uint32_t voff, rsrc_t srd, uint32_t soff) {
  asm volatile("s_mov_b32 m0, %0\n\ts_nop 0\n\tbuffer_load_dwordx4 %1, %2, %3 offen lds"
               :: "s"(m0v), "v"(voff), "s"(srd), "s"(soff) : "memory");
}
DEV void dma16_sc1(uint32_t m0v, uint32_t voff, rsrc_t srd, uint32_t soff) {
  asm volatile("s_mov_b32 m0, %0\n\ts_nop 0\n\tbuffer_load_dwordx4 %1, %2, %3 offen sc1 lds"
               :: "s"(m0v), "v"(voff), "s"(srd), "s"(soff) : "memory");
}
DEV void dma16_if(uint32_t cond, uint32_t m0v, uint32_t voff, rsrc_t srd, uint32_t soff) {
  asm volatile("s_cmp_eq_u32 %4, 0\n\ts_cbranch_scc1 1f\n\ts_mov_b32 m0, %0\n\ts_nop 0\n\t"
               "buffer_load_dwordx4 %1, %2, %3 offen sc1 lds\n1:"
               :: "s"(m0v), "v"(voff), "s"(srd), "s"(soff), "s"(cond) : "memory", "scc");
}
template <int N> DEV void wait_vm() { asm volatile("s_waitcnt vmcnt(%0)" :: "n"(N) : "memory"); }
DEV void bar() { asm volatile("s_waitcnt lgkmcnt(0)\n\ts_barrier" ::: "memory"); }
template <int N> DEV void open_stage() {
  asm volatile("s_waitcnt vmcnt(%0) lgkmcnt(0)\n\ts_barrier" :: "n"(N) : "memory");
}
template <int N0, int N1> DEV void open_stage2(uint32_t sel) {
  asm volatile("s_cmp_eq_u32 %2, 0\n\ts_cbranch_scc1 1f\n\ts_waitcnt vmcnt(%1)\n1:\n\ts_waitcnt vmcnt(%0) lgkmcnt(0)\n\ts_barrier"
               :: "n"(N0), "n"(N1), "s"(sel) : "memory", "scc");
}
constexpr uint32_t PF = MISC + 40960 - 32;
struct Sync { uint32_t g; uint32_t pfr; u64 mask; };
template <int N0, int N1> DEV void arrive2(const Sync& y, uint32_t sel) {
  uint32_t t;
  asm volatile("s_cmp_eq_u32 %6, 0\n\ts_cbranch_scc1 1f\n\ts_waitcnt vmcnt(%5)\n1:\n\ts_waitcnt vmcnt(%4)\n\tv_mov_b32 %0, %1\n\t"
               "s_mov_b64 exec, %2\n\tds_write_b32 %3, %0\n\ts_mov_b64 exec, -1"
               : "=&v"(t) : "s"(y.g), "s"(y.mask), "v"(y.pfr), "n"(N0), "n"(N1), "s"(sel) : "memory", "scc");
}
template <int N> DEV void arrive(const Sync& y) {
  uint32_t t;
  asm volatile("s_waitcnt vmcnt(%4)\n\tv_mov_b32 %0, %1\n\ts_mov_b64 exec, %2\n\tds_write_b32 %3, %0\n\ts_mov_b64 exec, -1"
               : "=&v"(t) : "s"(y.g), "s"(y.mask), "v"(y.pfr), "n"(N) : "memory");
}
DEV uint32_t flagread(const Sync& y) {
  uint32_t f; asm volatile("ds_read_b32 %0, %1" : "=v"(f) : "v"(y.pfr) : "memory"); return f;
}
DEV void check(Sync& y, uint32_t f) {
  asm volatile("s_waitcnt lgkmcnt(0)\n\tv_cmp_gt_u32 vcc, %1, %0\n\ts_cbranch_vccz 2f\n1:\n\tds_read_b32 %0, %2\n\t"
               "s_waitcnt lgkmcnt(0)\n\tv_cmp_gt_u32 vcc, %1, %0\n\ts_cbranch_vccnz 1b\n2:"
               : "+v"(f) : "s"(y.g), "v"(y.pfr) : "memory", "vcc");
  y.g += 1;
}
DEV void publish_if(uint32_t cond, rsrc_t r, uint32_t soff, u64 val) {
  u64 t;
  asm volatile("s_cmp_eq_u32 %4, 0\n\ts_cbranch_scc1 1f\n\tv_mov_b64 %0, %1\n\ts_mov_b64 exec, 1\n\t"
               "buffer_store_dwordx2 %0, off, %2, %3 sc1\n\ts_mov_b64 exec, -1\n1:"
               : "=&v"(t) : "s"(val), "s"(r), "s"(soff), "s"(cond) : "memory", "scc");
}
typedef AS3 char* lptr;
DEV lptr lp(uint32_t a) { asm volatile("" : "+v"(a)); return (lptr)a; }
DEV bf16x4 trd(lptr a) { return __builtin_amdgcn_ds_read_tr16_b64_v4bf16((AS3 bf16x4*)a); }
DEV bf16x8 trd2(lptr a0, lptr a1) {
  bf16x4 lo = trd(a0), hi = trd(a1);
  return __builtin_shufflevector(lo, hi, 0, 1, 2, 3, 4, 5, 6, 7);
}
DEV u32x4 lds_r128(lptr a) { return *(AS3 u32x4*)a; }
DEV f32x4 lds_rf4(lptr a) { return *(AS3 f32x4*)a; }
DEV void lds_w128(lptr a, u32x4 v) { *(AS3 u32x4*)a = v; }
DEV float lds_rf(lptr a) { return *(AS3 float*)a; }
DEV void lds_wf(lptr a, float v) { *(AS3 float*)a = v; }
DEV int opq(int v) { asm volatile("" : "+v"(v)); return v; }
DEV uint32_t opqs(uint32_t v) { asm volatile("" : "+s"(v)); return v; }
DEV uint32_t pk2(float a, float b) {
  f32x2 v = {a, b};
  return __builtin_bit_cast(uint32_t, __builtin_convertvector(v, bf16x2));
}
DEV float blo(uint32_t u) { return __builtin_bit_cast(float, u << 16); }
DEV float bhi(uint32_t u) { return __builtin_bit_cast(float, u & 0xffff0000u); }
DEV float phi(float t) {
  float e = __builtin_amdgcn_exp2f(t * 1.4426950408889634f);
  e = __builtin_amdgcn_fmed3f(e, 0.f, 1.f);
  return fmaxf(t, 0.f) + e + 1e-6f;
}
#define MFMA32(a, b, c) __builtin_amdgcn_mfma_f32_32x32x16_bf16(a, b, c, 0, 0, 0)
#define MFMA16(a, b, c) __builtin_amdgcn_mfma_f32_16x16x32_bf16(a, b, c, 0, 0, 0)
DEV bf16x8 asbf(u32x4 v) { return __builtin_bit_cast(bf16x8, v); }
DEV u32x4 pack8(const f32x16& a, int s) {
  u32x4 r;
  r[0] = pk2(a[8 * s + 0], a[8 * s + 1]); r[1] = pk2(a[8 * s + 2], a[8 * s + 3]);
  r[2] = pk2(a[8 * s + 4], a[8 * s + 5]); r[3] = pk2(a[8 * s + 6], a[8 * s + 7]);
  return r;
}
DEV void st128_sc1(u32x4 v, rsrc_t r, uint32_t voff, uint32_t soff) {
  __builtin_amdgcn_raw_buffer_store_b128(v, r, voff, soff, 16);
}
DEV void flag_store(char* ws, uint32_t off, u64 v) {
  __hip_atomic_store((AS1 u64*)(ws + off), v, __ATOMIC_RELAXED, __HIP_MEMORY_SCOPE_AGENT);
}
DEV u64 flag_load(char* ws, uint32_t off) {
  return __hip_atomic_load((AS1 u64*)(ws + off), __ATOMIC_RELAXED, __HIP_MEMORY_SCOPE_AGENT);
}
DEV bool flag_ok(u64 v, u64 tag, uint32_t lvl) { return ((v ^ tag) >> 8) == 0 && (uint32_t)(v & 0xff) >= lvl; }
DEV u32x2 aload64(rsrc_t r, uint32_t voff, uint32_t soff) {
  u32x2 v; asm volatile("buffer_load_dwordx2 %0, %1, %2, %3 offen sc1" : "=v"(v) : "v"(voff), "s"(r), "s"(soff) : "memory"); return v;
}
DEV u32x4 aload128(rsrc_t r, uint32_t voff, uint32_t soff) {
  u32x4 v; asm volatile("buffer_load_dwordx4 %0, %1, %2, %3 offen sc1" : "=v"(v) : "v"(voff), "s"(r), "s"(soff) : "memory"); return v;
}
DEV u32x4 aload128p(rsrc_t r, uint32_t voff, uint32_t soff) {
  u32x4 v; asm volatile("buffer_load_dwordx4 %0, %1, %2, %3 offen" : "=v"(v) : "v"(voff), "s"(r), "s"(soff) : "memory"); return v;
}
template <typename T> DEV void fence_reg(T& v) { asm volatile("" : "+v"(v) :: "memory"); }
DEV u64 u64of(u32x2 v) { return ((u64)v[1] << 32) | v[0]; }
DEV void poll_wait(char* ws, uint32_t off, uint32_t lvl, u64 tag) {
  for (uint32_t it = 0; it < SPIN_CAP; ++it) {
    u64 v = flag_load(ws, off);
    if (__all(flag_ok(v, tag, lvl))) break;
    __builtin_amdgcn_s_sleep(1);
  }
}
DEV void check_or_wait(u32x2 got, char* ws, uint32_t off, uint32_t lvl, u64 tag) {
  if (!__all(flag_ok(u64of(got), tag, lvl))) poll_wait(ws, off, lvl, tag);
}
#define STAMP(i) do { if (tid == 0) { ((u64*)(ws + (14u << 20)))[blockIdx.x * 32 + (i)] = __builtin_amdgcn_s_memrealtime(); ((u64*)(ws + (15u << 20)))[blockIdx.x * 32 + (i)] = __builtin_amdgcn_s_memtime(); } } while (0)
#define NDOT 4

#define NORM1(R) "v_lshlrev_b32 %1, 16, v" #R "\n\tv_and_b32 v" #R ", 0xffff0000, v" #R "\n\tv_fma_f32 %1, %1, %2, %3\n\tv_fma_f32 v" #R ", v" #R ", %2, %3\n\tv_cvt_pk_bf16_f32 v" #R ", %1, v" #R "\n\t"
#define NORMX(I, A, B, C, D) case I: asm volatile("s_cmp_eq_u32 %4, 0\n\ts_cbranch_scc1 1f\n\t" NORM1(A) NORM1(B) NORM1(C) NORM1(D) "s_nop 0\n1:" \
    : "+{v[" #A ":" #D "]}"(f), "=&v"(t) : "v"(rs), "v"(nb), "s"(cond) : "scc"); break;
DEV void norm_frag_if(uint32_t cond, int i, u32x4& f, float rs, float nb) {
  float t;
  switch (i) {
    NORMX(0, 96, 97, 98, 99) NORMX(1, 100, 101, 102, 103) NORMX(2, 104, 105, 106, 107) NORMX(3, 108, 109, 110, 111)
    NORMX(4, 112, 113, 114, 115) NORMX(5, 116, 117, 118, 119) NORMX(6, 120, 121, 122, 123) NORMX(7, 124, 125, 126, 127)
    NORMX(8, 128, 129, 130, 131) NORMX(9, 132, 133, 134, 135) NORMX(10, 136, 137, 138, 139) NORMX(11, 140, 141, 142, 143)
    NORMX(12, 144, 145, 146, 147) NORMX(13, 148, 149, 150, 151) NORMX(14, 152, 153, 154, 155) NORMX(15, 156, 157, 158, 159)
    NORMX(16, 160, 161, 162, 163) NORMX(17, 164, 165, 166, 167) NORMX(18, 168, 169, 170, 171) NORMX(19, 172, 173, 174, 175)
    NORMX(20, 176, 177, 178, 179) NORMX(21, 180, 181, 182, 183) NORMX(22, 184, 185, 186, 187) NORMX(23, 188, 189, 190, 191)
    NORMX(24, 192, 193, 194, 195) NORMX(25, 196, 197, 198, 199) NORMX(26, 200, 201, 202, 203) NORMX(27, 204, 205, 206, 207)
    NORMX(28, 208, 209, 210, 211) NORMX(29, 212, 213, 214, 215) NORMX(30, 216, 217, 218, 219) NORMX(31, 220, 221, 222, 223)
    NORMX(32, 224, 225, 226, 227) NORMX(33, 228, 229, 230, 231) NORMX(34, 232, 233, 234, 235) NORMX(35, 236, 237, 238, 239)
    NORMX(36, 240, 241, 242, 243) NORMX(37, 244, 245, 246, 247) NORMX(38, 248, 249, 250, 251) NORMX(39, 252, 253, 254, 255)
  }
}
DEV uint32_t nxt(uint32_t s) { return s + SLOT == RING ? 0u : s + SLOT; }

DEV uint32_t kvq_voff(int piece, int lane, int i, bool isq) {
  uint32_t p = piece * 1024 + lane * 16;
  uint32_t row = (p * 52429u) >> 24;
  uint32_t col = p - row * 320;
  bool hi = col >= 160;
  uint32_t c = hi ? col - 160 : col;
  return isq ? (row + (hi ? 64u : 0u)) * 3840 + 160 * i + c
             : row * 3840 + (hi ? 2560u : 1280u) + 160 * i + c;
}
DEV void check4(u32x4 ft0, u32x4 ft1, char* ws, uint32_t off, u64 tag) {
  u32x2 f0 = {ft0[0], ft0[1]}, f1 = {ft0[2], ft0[3]}, f2 = {ft1[0], ft1[1]}, f3 = {ft1[2], ft1[3]};
  bool ok = flag_ok(u64of(f0), tag, 1) && flag_ok(u64of(f1), tag, 1) && flag_ok(u64of(f2), tag, 1) && flag_ok(u64of(f3), tag, 1);
  if (!__all(ok)) {
#pragma unroll 1
    for (int j = 0; j < 4; ++j) poll_wait(ws, off + 8 * j, 1, tag);
  }
}

template <int NH>
DEV void kv_chunk(u32x4 (&xf)[40], float rs, float nb, uint32_t& cur, uint32_t& prv, int tid, int w, int ci,
                  uint32_t kv0, uint32_t kv1, uint32_t kv2, uint32_t guvo, rsrc_t rqkv, rsrc_t rg_, rsrc_t ru_) {
  const int l = tid & 63;
  const uint32_t ta = (8 * (l >> 5) + ((l >> 2) & 3)) * 320 + ((l >> 4) & 1) * 32 + 8 * (l & 3) + NH * 192;
  f32x16 a0 = {}, a1 = {}, a2 = {};
  bf16x8 w0, w1, w2, n0, n1, n2;
  norm_frag_if(1, 0, xf[0], rs, nb);
  open_stage<8>();
  lptr base = lp(cur + ta);
  w0 = trd2(base, base + 1280); w1 = trd2(base + 64, base + 64 + 1280);
  if (NH == 0) w2 = trd2(base + 128, base + 128 + 1280);
#pragma unroll
  for (int s = 0; s < 10; ++s) {
#pragma unroll
    for (int t = 0; t < 4; ++t) {
      const int ks = 4 * s + t;
      SB0();
      if (s < 5) {
        if (t == 0) dma16(prv + w * 1024, kv0, rqkv, (s + 5) * 64 * 3840);
        if (t == 1) dma16(prv + (w + 8) * 1024, kv1, rqkv, (s + 5) * 64 * 3840);
        if (t == 2 && NH == 0) dma16(prv + (w + 16) * 1024, kv2, rqkv, (s + 5) * 64 * 3840);
      } else {
        if (t == 0) dma16(prv + w * 1024, guvo, rg_, (s - 5) * 64 * 3072 + (16 + ci) * 128);
        if (t == 1) dma16(prv + (w + 8) * 1024, guvo, ru_, (s - 5) * 64 * 3072 + (16 + ci) * 128);
      }
      if (t < 3 || s < 9) {
        if (t == 3) { open_stage<8>(); prv = cur; cur = nxt(cur); base = lp(cur + ta); }
        const lptr o = base + ((t + 1) & 3) * 5120;
        n0 = trd2(o, o + 1280); n1 = trd2(o + 64, o + 64 + 1280);
        if (NH == 0) n2 = trd2(o + 128, o + 128 + 1280);
      }
      SB0();
      a0 = MFMA32(asbf(xf[ks]), w0, a0);
      a1 = MFMA32(asbf(xf[ks]), w1, a1);
      if (NH == 0) a2 = MFMA32(asbf(xf[ks]), w2, a2);
      SB0();
      if (ks < 39) norm_frag_if(1, ks + 1, xf[ks + 1], rs, nb);
      w0 = n0; w1 = n1; if (NH == 0) w2 = n2;
    }
  }
  SB0();
  const int l2 = opq(tid) & 63;
  if (NH == 0) {
#pragma unroll
    for (int j = 0; j < 16; ++j) { a0[j] = phi(a0[j]); a1[j] = phi(a1[j]); float p = phi(a2[j]); a2[j] = (l2 & 16) ? a2[j] : p; }
  }
  const lptr e0 = lp(MISC + ((NH * 12 + (w & 3)) * 2) * 1024 + l2 * 16);
  lds_w128(e0, pack8(a0, 0)); lds_w128(e0 + 1024, pack8(a0, 1));
  lds_w128(e0 + 8192, pack8(a1, 0)); lds_w128(e0 + 8192 + 1024, pack8(a1, 1));
  if (NH == 0) { lds_w128(e0 + 16384, pack8(a2, 0)); lds_w128(e0 + 16384 + 1024, pack8(a2, 1)); }
}

template <int NH>
DEV void q_chunk(u32x4 (&xf)[40], uint32_t& cur, uint32_t& prv, int tid, int w, int ci, int rg, int ct, uint32_t fb_row,
                 rsrc_t rws, char* ws, u64 tag) {
  const int l = tid & 63, rb = w & 3;
  const uint32_t ta = (8 * (l >> 5) + ((l >> 2) & 3)) * 320 + ((l >> 4) & 1) * 32 + 8 * (l & 3) + NH * 128;
  const uint32_t ZP = MISC, ZS = MISC + 5120, X3 = MISC + 5632;
  const uint32_t l16 = l * 16;
  f32x16 a0 = {}, a1 = {};
  bf16x8 w0, w1, n0, n1;
  open_stage<8>();
  const int zt = min(tid, 319), zr = (zt * 3277) >> 16;
  u32x4 zp = aload128(rws, zr * KVREC + (zt - 20 * zr) * 16, WS_KVP + ci * 16 * KVREC + 6400);
  lptr base = lp(cur + ta);
  w0 = trd2(base, base + 1280);
  if (NH == 0) w1 = trd2(base + 64, base + 64 + 1280);
#pragma unroll
  for (int s = 0; s < 5; ++s) {
#pragma unroll
    for (int t = 0; t < 8; ++t) {
      const int ks = 8 * s + t;
      SB0();
      if (t < 2 || (t == 2 && NH == 0))
        dma16_sc1(prv + (w + 8 * t) * 1024, l16, rws, WS_W2 + (ct * 340 + s * 20 + w + 8 * t) * 1024);
      if (s == 2 && t == 0) {
        if (NH == 0 && tid == 0) flag_store(ws, WS_FB + rg * 128 + ci * 8, (tag & ~0xffull) | 2);
        fence_reg(zp); lds_w128(lp(ZP + zt * 16), zp);
      }
      if (NH == 1 && s == 3 && t == 0 && w < 6) {
        const int zi = min(tid - 256, 79);
        const lptr za = lp(ZP + zi * 4);
        float z = 0.f;
#pragma unroll
        for (int j = 0; j < 16; ++j) z += lds_rf(za + j * 320);
        lds_wf(za + 5120, z);
      }
      if (t < 7 || s < 4) {
        if (t == 7) {
          if (s == 1) open_stage<4>(); else open_stage<8>();
          prv = cur; cur = nxt(cur); base = lp(cur + ta);
        }
        const int tn = (t + 1) & 7;
        const lptr o = base + (tn & 3) * 5120 + (tn >> 2) * 160;
        n0 = trd2(o, o + 1280);
        if (NH == 0) n1 = trd2(o + 64, o + 64 + 1280);
      }
      SB0();
      a0 = MFMA32(w0, asbf(xf[ks]), a0);
      if (NH == 0) a1 = MFMA32(w1, asbf(xf[ks]), a1);
      w0 = n0; if (NH == 0) w1 = n1;
    }
  }
  SB0();
  prv = cur; cur = nxt(cur);
  STAMP(8);
  const int l2 = opq(tid) & 63, r = l2 & 31, hh = l2 >> 5;
  const uint32_t pv = (r >> 4) * (68 * 1024) + (hh * 16 + (r & 15)) * 16;
  const uint32_t qb = WS_A2 + ((rg * 8 + 2 * rb) * 68 + 48) * 1024 + ci * 5 * 512;
  const lptr zs = lp(ZS + 16 * hh), x3 = lp(X3 + (32 * rb + r) * 4);
  if (NH == 0) {
    u32x2 pl = aload64(rws, (l2 & 7) * 8, fb_row);
    float gs[8];
#pragma unroll
    for (int g = 0; g < 8; ++g) {
      const f32x4 z = lds_rf4(zs + 32 * g);
      float acc = 0.f;
#pragma unroll
      for (int j = 0; j < 4; ++j) {
        float v = phi(g < 4 ? a0[4 * g + j] : a1[4 * (g - 4) + j]);
        if (g < 4) a0[4 * g + j] = v; else a1[4 * (g - 4) + j] = v;
        acc = fmaf(v, z[j], acc);
      }
      gs[g] = acc;
    }
    float p0 = gs[0] + gs[1] + (hh ? 0.f : gs[2]);
    float p1 = gs[3] + gs[4] + (hh ? gs[2] : 0.f);
    float p2 = gs[5] + gs[6] + (hh ? 0.f : gs[7]);
    if (hh) lds_wf(x3, gs[7]);
    p0 += __shfl_xor(p0, 32); p1 += __shfl_xor(p1, 32); p2 += __shfl_xor(p2, 32);
    wait_vm<0>(); fence_reg(pl);
    if (w == 0) check_or_wait(pl, ws, fb_row + (l2 & 7) * 8, 2, tag);
    bar();
    const float p3 = gs[7] + lds_rf(x3 + 512);
    const float r0 = 1.f / (p0 + 1e-6f), r1 = 1.f / (p1 + 1e-6f), r2 = 1.f / (p2 + 1e-6f), r3 = 1.f / (p3 + 1e-6f);
    const float sc[8] = {r0, r0, hh ? r1 : r0, r1, r1, r2, r2, hh ? r3 : r2};
#pragma unroll
    for (int j = 0; j < 16; ++j) { a0[j] = a0[j] * sc[j >> 2]; a1[j] = a1[j] * sc[4 + (j >> 2)]; }
    st128_sc1(pack8(a0, 0), rws, pv, qb); st128_sc1(pack8(a0, 1), rws, pv, qb + 512);
    st128_sc1(pack8(a1, 0), rws, pv, qb + 1024); st128_sc1(pack8(a1, 1), rws, pv, qb + 1536);
  } else {
    float p3 = 0.f;
#pragma unroll
    for (int g = 0; g < 2; ++g) {
      const f32x4 z = lds_rf4(zs + 256 + 32 * g);
#pragma unroll
      for (int j = 0; j < 4; ++j) { float v = phi(a0[4 * g + j]); a0[4 * g + j] = v; p3 = fmaf(v, z[j], p3); }
    }
    p3 += __shfl_xor(p3, 32);
    if (hh) lds_wf(x3 + 512, p3);
    bar();
    p3 += lds_rf(x3);
    const float r3 = 1.f / (p3 + 1e-6f);
#pragma unroll
    for (int j = 0; j < 8; ++j) a0[j] = a0[j] * r3;
    st128_sc1(pack8(a0, 0), rws, pv, qb + 2048);
  }
}

__global__ __launch_bounds__(512, 2) void fused_kernel(const void* __restrict__ gx, const void* __restrict__ gwqkv,
                                                       const void* __restrict__ gwo, const void* __restrict__ gwg,
                                                       const void* __restrict__ gwu, const void* __restrict__ gwd,
                                                       void* __restrict__ gout, char* __restrict__ ws) {
  const int tid = threadIdx.x;
  const int w = __builtin_amdgcn_readfirstlane(tid >> 6);
  const int rb = w & 3, nh = w >> 2;
  const int b = blockIdx.x;
  const int xcd = b & 7, idx = b >> 3;
  const int rg = (xcd >> 2) * 8 + (idx >> 2);
  const bool iskv = (idx & 2) != 0;
  const int ci = 2 * (xcd & 3) + (idx & 1);
  const int tt = (xcd >> 1) * 8 + (idx >> 2), ct = (xcd & 1) * 4 + (idx & 3);

  const rsrc_t rx = mk_rsrc(gx, 2048 * 1280), rqkv = mk_rsrc(gwqkv, 640 * 3840);
  const rsrc_t rg_ = mk_rsrc(gwg, 640 * 3072), ru_ = mk_rsrc(gwu, 640 * 3072);
  const rsrc_t rws = mk_rsrc(ws, WS_USED);
  STAMP(0);

  uint32_t cur = 0, prv = 5 * SLOT;
  u64 tag;
  u32x4 xf[40];
  float rs, nb;
  uint32_t guvo;
  {
    const int l = tid & 63, hh = l >> 5, r = l & 31;
    u32x2 eld = aload64(rws, 0, WS_E);
    const int t0ks = b / 5, t0cg = b - 5 * t0ks;
    if (b < 240) dma16(MISC + w * 1024, (4 * w + (l >> 4)) * 1280 + (l & 15) * 16, mk_rsrc(gwd, 1536 * 1280), t0ks * 32 * 1280 + t0cg * 256);
    const uint32_t xvo = (8 * w + (l >> 3)) * 1280 + (((l & 7) ^ ((4 * w + (l >> 4)) & 7)) << 4);
    const uint32_t xso = rg * 128 * 1280;
#pragma unroll
    for (int s = 0; s < 5; ++s) {
      dma16(s * SLOT + w * 1024, xvo, rx, xso + s * 128);
      dma16(s * SLOT + (w + 8) * 1024, xvo, rx, xso + s * 128 + 64 * 1280);
    }
    guvo = (8 * w + (l >> 3)) * 3072 + (((l & 7) ^ (((l >> 4) & 1) << 2)) << 4);
    const uint32_t xra = (32 * rb + r) * 128 + ((hh ^ ((r >> 1) & 7)) << 4);
    float s0 = 0.f, s1 = 0.f, q0 = 0.f, q1 = 0.f;
    const uint32_t kv0 = iskv ? kvq_voff(w, l, ci, false) : guvo, kv1 = iskv ? kvq_voff(w + 8, l, ci, false) : guvo;
    const uint32_t kv2 = kvq_voff((w & 3) + 16, l, ci, false);
    const rsrc_t rf0 = iskv ? rqkv : rg_, rf1 = iskv ? rqkv : ru_;
    const uint32_t fstr = iskv ? 64 * 3840 : 64 * 3072, fso = iskv ? 0 : ci * 128, third = ((idx >> 1) & 1) & (nh ^ 1);
#pragma unroll
    for (int s = 0; s < 10; ++s) {
      open_stage<8>();
#pragma unroll
      for (int t = 0; t < 4; ++t) xf[4 * s + t] = lds_r128((lptr)(cur + (xra ^ (t << 5))));
      if (s < 5) {
        dma16(prv + w * 1024, xvo, rx, xso + (s + 5) * 128);
        dma16(prv + (w + 8) * 1024, xvo, rx, xso + (s + 5) * 128 + 64 * 1280);
      } else {
        dma16(prv + w * 1024, kv0, rf0, fso + (s - 5) * fstr);
        dma16(prv + (w + 8) * 1024, kv1, rf1, fso + (s - 5) * fstr);
        dma16_if(third, prv + (w + 16) * 1024, kv2, rqkv, (s - 5) * 64 * 3840);
      }
      if (s == 0) {
        fence_reg(eld);
        u64 e = ((u64)(uint32_t)__builtin_amdgcn_readfirstlane(eld[1]) << 32) | (uint32_t)__builtin_amdgcn_readfirstlane(eld[0]);
        tag = (e * 6364136223846793005ull + 1442695040888963407ull);
        if (b < 240) {
          const int g = l >> 4;
          const lptr a = lp(MISC + (16 * (g >> 1) + 4 * (g & 1) + ((l >> 2) & 3)) * 256 + w * 32 + 8 * (l & 3));
          bf16x8 f = trd2(a, a + 8 * 256);
          const int cb = 8 * t0cg + w, c5 = cb / 5, n5 = cb - 5 * c5;
          st128_sc1(__builtin_bit_cast(u32x4, f), rws, l * 16, WS_W2 + ((c5 * 68 + t0ks) * 5 + n5) * 1024);
        }
      }
      if (s == 6 && tid == 0) flag_store(ws, WS_FT + b * 8, (tag & ~0xffull) | 1);
#pragma unroll
      for (int t = 0; t < NDOT; ++t) {
        const uint32_t e0 = xf[4 * s + t][0], e1 = xf[4 * s + t][1], e2 = xf[4 * s + t][2], e3 = xf[4 * s + t][3];
        asm volatile("v_dot2c_f32_bf16 %0, %8, %4\n\tv_dot2c_f32_bf16 %2, %4, %4\n\tv_dot2c_f32_bf16 %1, %8, %5\n\tv_dot2c_f32_bf16 %3, %5, %5\n\t"
                     "v_dot2c_f32_bf16 %0, %8, %6\n\tv_dot2c_f32_bf16 %2, %6, %6\n\tv_dot2c_f32_bf16 %1, %8, %7\n\tv_dot2c_f32_bf16 %3, %7, %7"
                     : "+v"(s0), "+v"(s1), "+v"(q0), "+v"(q1) : "v"(e0), "v"(e1), "v"(e2), "v"(e3), "s"(0x3f803f80u));
      }
      SB0();
      prv = cur; cur = nxt(cur);
    }
    asm volatile("s_nop 3" : "+v"(s0), "+v"(s1), "+v"(q0), "+v"(q1));
    float sm = s0 + s1, sq = q0 + q1;
    sm += __shfl_xor(sm, 32); sq += __shfl_xor(sq, 32);
    const float mean = sm * (1.f / 640.f);
    const float var = fmaxf(sq * (1.f / 640.f) - mean * mean, 0.f);
    rs = 1.0f / sqrtf(var + 1e-5f);
    nb = -mean * rs;
    STAMP(1);

    if (iskv) {
      if (nh == 0) kv_chunk<0>(xf, rs, nb, cur, prv, opq(tid), w, ci, kv0, kv1, kv2, guvo, rqkv, rg_, ru_);
      if (opqs(nh) != 0) kv_chunk<1>(xf, rs, nb, cur, prv, opq(tid), w, ci, kv0, kv1, kv2, guvo, rqkv, rg_, ru_);
      STAMP(2);
      const int t2 = opq(tid), l = t2 & 63, hh = l >> 5, r = l & 31;
      u32x4 ft0, ft1;
      if (w == 7) { ft0 = aload128(rws, l * 32, WS_FT); ft1 = aload128(rws, l * 32, WS_FT + 16); }
      bar();
      const int kb = w < 5 ? (w >> 1) : (w - 5), vb = 2 + ((w + 1) >> 1);
      const lptr ka = lp(MISC + kb * 8192 + l * 16), va = lp(MISC + vb * 8192 + l * 16);
      f32x16 d = {};
      const uint32_t ck = cur;
      if (w < 5) {
#pragma unroll
        for (int j = 0; j < 8; ++j) d = MFMA32(asbf(lds_r128(ka + j * 1024)), asbf(lds_r128(va + j * 1024)), d);
        const int ve = 32 * vb + r - 80;
        const int hv = (ve * 205) >> 12;
        const int e = ve - 20 * hv;
        const int lo = 20 * hv - 32 * kb - 4 * hh;
        const uint32_t ca = ck + (hv * 400 + e - lo * 20) * 4;
#pragma unroll
        for (int j = 0; j < 16; ++j) {
          const int rowb = (j & 3) + 8 * (j >> 2);
          if (ve >= 0 && (unsigned)(rowb - lo) < 20u) lds_wf((lptr)(ca + rowb * 80), d[j]);
        }
      } else {
        const u32x4 ones = {0x3f803f80u, 0x3f803f80u, 0x3f803f80u, 0x3f803f80u};
#pragma unroll
        for (int j = 0; j < 8; ++j) d = MFMA32(asbf(ones), asbf(lds_r128(ka + j * 1024)), d);
        if (hh == 0 && 32 * kb + r < 80) lds_wf((lptr)(ck + 6400 + (32 * kb + r) * 4), d[0]);
      }
      if (w == 7) { wait_vm<0>(); fence_reg(ft0); fence_reg(ft1); check4(ft0, ft1, ws, WS_FT + l * 32, tag); }
      bar();
      if (t2 < 420) st128_sc1(lds_r128((lptr)(ck + t2 * 16)), rws, t2 * 16, WS_KVP + (ci * 16 + rg) * KVREC);
      prv = cur; cur = nxt(cur);
      STAMP(3);
    }
  }

  {
    const int ngu = iskv ? 1 : 2;
#pragma unroll 1
    for (int it = 0; it < ngu; ++it) {
      const int l = opq(tid) & 63;
      const uint32_t ga = (8 * (l >> 5) + ((l >> 2) & 3)) * 128 + 64 * (nh ^ ((l >> 3) & 1)) + 32 * ((l >> 4) & 1) + 8 * (l & 3);
      const uint32_t first = opqs((((idx >> 1) | it) & 1) ^ 1);
      const int c = iskv ? 16 + ci : it * 8 + ci;
      uint32_t nv0, nv1, nv2, nso, nstr; rsrc_t nr0, nr1;
      if (first) { nv0 = nv1 = nv2 = guvo; nr0 = rg_; nr1 = ru_; nso = (c + 8) * 128; nstr = 64 * 3072; }
      else if (!iskv) { nv0 = kvq_voff(w, l, ci, true); nv1 = kvq_voff(w + 8, l, ci, true); nv2 = kvq_voff((w & 3) + 16, l, ci, true);
                        nr0 = nr1 = rqkv; nso = 0; nstr = 128 * 3840; }
      else { nv0 = l * 16; nv1 = l * 16 + 8192; nv2 = l * 16 + 16384; nr0 = nr1 = rws; nso = WS_W2 + (ct * 340 + w) * 1024; nstr = 20480; }
      nso = __builtin_amdgcn_readfirstlane(nso); nstr = __builtin_amdgcn_readfirstlane(nstr);
      const uint32_t third = (first | nh) ^ 1;
      const uint32_t gso = opqs(c * 128);
      f32x16 ag = {}, au = {};
      bf16x8 w0, w1, n0, n1;
      norm_frag_if(first, 0, xf[0], rs, nb);
      open_stage<8>();
      lptr base = lp(cur + ga);
      w0 = trd2(base, base + 512); w1 = trd2(base + 8192, base + 8192 + 512);
#pragma unroll
      for (int s = 0; s < 10; ++s) {
#pragma unroll
        for (int t = 0; t < 4; ++t) {
          const int ks = 4 * s + t;
          SB0();
          if (s < 5) {
            if (t == 0) dma16(prv + w * 1024, guvo, rg_, (s + 5) * 64 * 3072 + gso);
            if (t == 1) dma16(prv + (w + 8) * 1024, guvo, ru_, (s + 5) * 64 * 3072 + gso);
          } else {
            if (t == 0) dma16_sc1(prv + w * 1024, nv0, nr0, nso);
            if (t == 1) dma16_sc1(prv + (w + 8) * 1024, nv1, nr1, nso);
            if (t == 2) { dma16_if(third, prv + (w + 16) * 1024, nv2, nr1, nso); nso += nstr; }
          }
          if (s == 4 && t == 0 && iskv && tid == 0) flag_store(ws, WS_FA + (ci * 16 + rg) * 8, (tag & ~0xffull) | 1);
          if (t < 3 || s < 9) {
            if (t == 3) {
 open_stage<8>();
 prv = cur; cur = nxt(cur); base = lp(cur + ga); }
            const lptr o = base + ((t + 1) & 3) * 2048;
            n0 = trd2(o, o + 512); n1 = trd2(o + 8192, o + 8192 + 512);
          }
          SB0();
          ag = MFMA32(w0, asbf(xf[ks]), ag);
          au = MFMA32(w1, asbf(xf[ks]), au);
          SB0();
          if (ks < 39) norm_frag_if(first, ks + 1, xf[ks + 1], rs, nb);
          w0 = n0; w1 = n1;
        }
      }
      SB0();
      prv = cur; cur = nxt(cur);
      STAMP(4 + 2 * it);
      const int l2 = opq(tid) & 63, r = l2 & 31, hh = l2 >> 5;
      const uint32_t fvo = first ? l2 * 32 : (l2 & 3) * 32, fso = first ? WS_FT : WS_FA + ci * 128;
      u32x4 ft0, ft1;
      if (w == 0) { ft0 = aload128(rws, fvo, fso); ft1 = aload128(rws, fvo, fso + 16); }
#pragma unroll
      for (int j = 0; j < 16; ++j) ag[j] = ag[j] * au[j];
      const uint32_t hv = (r >> 4) * (68 * 1024) + (hh * 16 + (r & 15)) * 16;
      const uint32_t hs = WS_A2 + ((rg * 8 + 2 * rb) * 68 + 2 * c + nh) * 1024;
      st128_sc1(pack8(ag, 0), rws, hv, hs);
      st128_sc1(pack8(ag, 1), rws, hv + 512, hs);
      if (w == 0) {
        wait_vm<2>(); fence_reg(ft0); fence_reg(ft1);
        check4(ft0, ft1, ws, fso + fvo, tag);
        if (b == 0 && first && l2 == 0) flag_store(ws, WS_E, tag);
      }
      STAMP(5 + 2 * it);
    }
  }

  uint32_t pub_off, pub_lvl;
  const uint32_t fb_row = WS_FB + (tt >> 1) * 128;
  if (!iskv) {
    if (nh == 0) q_chunk<0>(xf, cur, prv, opq(tid), w, ci, rg, ct, fb_row, rws, ws, tag);
    else q_chunk<1>(xf, cur, prv, opq(tid), w, ci, rg, ct, fb_row, rws, ws, tag);
    pub_off = WS_FB + rg * 128 + ci * 8; pub_lvl = 3;
  } else {
    const int hl = rg & 3, qd = rg >> 2, head = 4 * ci + hl;
    const int tid = opq(threadIdx.x), l = tid & 63, q4 = (l >> 2) & 3, p4 = l & 3;
    const uint32_t WO = MISC, RED = MISC + 6400;
    bar();
    u32x2 pl; u32x4 wo4, k0, k1, k2, k3;
    if (w == 0) pl = aload64(rws, (l & 7) * 8, fb_row);
    if (tid < 400) {
      const int row = (tid * 3277) >> 16, rq = (tid * 656) >> 16;
      wo4 = aload128p(mk_rsrc(gwo, 640 * 1280), row * 1280 + (tid - 20 * row) * 16, head * 20 * 1280 + qd * 320);
      const uint32_t vo = rq * 4 * KVREC + (tid - 100 * rq) * 16, so = WS_KVP + ci * 16 * KVREC + hl * 1600;
      k0 = aload128(rws, vo, so); k1 = aload128(rws, vo, so + KVREC);
      k2 = aload128(rws, vo, so + 2 * KVREC); k3 = aload128(rws, vo, so + 3 * KVREC);
    }
    wait_vm<0>();
    if (tid < 400) {
      fence_reg(wo4); fence_reg(k0); fence_reg(k1); fence_reg(k2); fence_reg(k3);
      lds_w128((lptr)(WO + tid * 16), wo4);
      f32x4 sum = (__builtin_bit_cast(f32x4, k0) + __builtin_bit_cast(f32x4, k1)) + (__builtin_bit_cast(f32x4, k2) + __builtin_bit_cast(f32x4, k3));
      lds_w128((lptr)(RED + tid * 16), __builtin_bit_cast(u32x4, sum));
    }
    if (w == 0) { fence_reg(pl); check_or_wait(pl, ws, fb_row + (l & 7) * 8, 2, tag); }
    bar();
    if (tid == 0) flag_store(ws, WS_FB + rg * 128 + (8 + ci) * 8, (tag & ~0xffull) | 1);
    STAMP(8);
    {
      const int i16 = l & 15, g = l >> 4;
      u32x4 af[2];
#pragma unroll
      for (int db = 0; db < 2; ++db) {
        const int d = min(16 * db + i16, 19);
        f32x4 lo = {}, hi = {};
        const lptr al = lp(RED + d * 80 + min(g, 2) * 32), ah = lp(RED + d * 80 + min(g, 2) * 32 + (g < 2 ? 16 : 0));
#pragma unroll
        for (int j = 0; j < 4; ++j) {
          lo += lds_rf4(al + j * 1600);
          hi += lds_rf4(ah + j * 1600);
        }
        af[db][0] = g < 3 ? pk2(lo[0], lo[1]) : 0u; af[db][1] = g < 3 ? pk2(lo[2], lo[3]) : 0u;
        af[db][2] = g < 2 ? pk2(hi[0], hi[1]) : 0u; af[db][3] = g < 2 ? pk2(hi[2], hi[3]) : 0u;
      }
      const uint32_t b0 = WO + min(8 * g + q4, 16 + q4) * 320 + 8 * p4, b1 = WO + min(8 * g + 4 + q4, 16 + q4) * 320 + 8 * p4;
      for (int n = w; n < 10; n += 8) {
        const bf16x8 bf = trd2((lptr)(b0 + n * 32), (lptr)(b1 + n * 32));
        const f32x4 z4 = {};
        const f32x4 m0 = MFMA16(asbf(af[0]), bf, z4), m1 = MFMA16(asbf(af[1]), bf, z4);
        const int cb = 10 * qd + n, c5 = cb / 5, n5 = cb - 5 * c5;
        const uint32_t fb = WS_W2 + (c5 * 340 + 240 + n5) * 1024 + i16 * 16;
        const int G0 = 5 * head + g, G1 = 5 * head + 4;
        const uint32_t o0 = fb + (G0 >> 3) * 5120 + (((G0 >> 2) & 1) * 2 + (G0 & 1)) * 256 + ((G0 >> 1) & 1) * 8;
        const uint32_t o1 = fb + (G1 >> 3) * 5120 + (((G1 >> 2) & 1) * 2 + (G1 & 1)) * 256 + ((G1 >> 1) & 1) * 8;
        u32x2 v0 = {pk2(m0[0], m0[1]), pk2(m0[2], m0[3])}, v1 = {pk2(m1[0], m1[1]), pk2(m1[2], m1[3])};
        __builtin_amdgcn_raw_buffer_store_b64(v0, rws, o0, 0, 16);
        if (g == 0) __builtin_amdgcn_raw_buffer_store_b64(v1, rws, o1, 0, 16);
      }
    }
    pub_off = WS_FM + (qd * 32 + head) * 8; pub_lvl = 1;
  }
  STAMP(9);

  {
    const int kh = nh;
    const int tid = opq(threadIdx.x), l = tid & 63;
    u32x4 A[34];
    const int pr0 = (tid * 6554) >> 16;
    const int pr1 = ((tid + 512) * 6554) >> 16;
    const uint32_t xo0 = pr0 * 1280 + (tid - 10 * pr0) * 16, xo1 = pr1 * 1280 + (tid + 512 - 10 * pr1) * 16;
    const uint32_t xs = tt * 64 * 1280 + ct * 160;
    u32x4 xr0 = aload128p(rx, xo0, xs), xr1;
    if (tid < 128) xr1 = aload128p(rx, xo1, xs);
    const uint32_t as = WS_A2 + ((tt * 4 + rb) * 68 + 2 * kh) * 1024;
    const uint32_t l16 = l * 16;
#pragma unroll
    for (int p = 0; p < 5; ++p) { A[2 * p] = aload128(rws, l16, as + p * 4096); A[2 * p + 1] = aload128(rws, l16, as + p * 4096 + 1024); }
    u32x2 pl;
    const uint32_t po2 = fb_row + 64 + (l & 7) * 8;
    const uint32_t po3 = l < 32 ? WS_FM + (ct >> 1) * 256 + l * 8 : fb_row + (l & 7) * 8;
    if (w == 0) pl = aload64(rws, po2, 0);
    f32x4 acc[5] = {};
#pragma unroll
    for (int p = 0; p < 17; ++p) {
      const int lo = p < 5 ? 0 : p - 4, hi = p - 1 < 11 ? p - 1 : 11;
      const int nop = hi >= lo ? hi - lo + 1 : 0, ex = p < 5 ? 2 * (4 - p) : 0;
      if (p == 3 || p == 7) {
        if (w == 0) {
          wait_vm<15>(); fence_reg(pl);
          if (p == 3) check_or_wait(pl, ws, po2, 1, tag); else check_or_wait(pl, ws, po3, l < 32 ? 1 : 3, tag);
        }
      }
      switch (nop * 16 + ex) {
#define OPEN_CASE(NOP, EX) case NOP * 16 + EX: open_stage2<NOP * 5 + EX, NOP * 4 + EX>(nh); break;
        OPEN_CASE(0, 8) OPEN_CASE(1, 6) OPEN_CASE(2, 4) OPEN_CASE(3, 2) OPEN_CASE(4, 0)
        OPEN_CASE(3, 0) OPEN_CASE(2, 0) OPEN_CASE(1, 0) OPEN_CASE(0, 0)
        default: open_stage<0>();
      }
      fence_reg(A[2 * p]); fence_reg(A[2 * p + 1]);
      if (p == 0 && tid == 0) flag_store(ws, pub_off, (tag & ~0xffull) | pub_lvl);
      const lptr rbs = lp(cur + kh * 10240 + l16);
      u32x4 B[10];
#pragma unroll
      for (int j = 0; j < 10; ++j) B[j] = lds_r128(rbs + j * 1024);
      SB0();
      if (p + 5 < 17) {
        const uint32_t so = WS_W2 + (ct * 340 + (p + 5) * 20 + w) * 1024;
        dma16_sc1(prv + w * 1024, l16, rws, so);
        dma16_sc1(prv + (w + 8) * 1024, l16, rws, so + 8192);
        dma16_if(nh ^ 1, prv + (w + 16) * 1024, l16, rws, so + 16384);
        A[2 * p + 10] = aload128(rws, l16, as + (p + 5) * 4096);
        A[2 * p + 11] = aload128(rws, l16, as + (p + 5) * 4096 + 1024);
      }
      if (p == 3 && w == 0) pl = aload64(rws, po3, 0);
      SB0();
#pragma unroll
      for (int j = 0; j < 10; ++j) acc[j % 5] = MFMA16(asbf(B[j]), asbf(A[2 * p + j / 5]), acc[j % 5]);
      SB0();
      prv = cur; cur = nxt(cur);
    }
    STAMP(10);
    bar();
    const lptr ow = lp((kh * 64 + rb * 16 + (l & 15)) * 336 + (l >> 4) * 16);
#pragma unroll
    for (int j = 0; j < 5; ++j) lds_w128(ow + j * 64, __builtin_bit_cast(u32x4, acc[j]));
    bar();
    const rsrc_t ro = mk_rsrc(gout, 2048 * 1280);
#pragma unroll
    for (int h = 0; h < 2; ++h) {
      if (h == 0 || tid < 128) {
        u32x4 xr = h ? xr1 : xr0;
        fence_reg(xr);
        const int pr = h ? pr1 : pr0, pc = tid + 512 * h - 10 * pr;
        const lptr a = lp(pr * 336 + pc * 32);
        f32x4 u0 = lds_rf4(a) + lds_rf4(a + 64 * 336);
        f32x4 u1 = lds_rf4(a + 16) + lds_rf4(a + 16 + 64 * 336);
        u32x4 o;
        o[0] = pk2(u0[0] + blo(xr[0]), u0[1] + bhi(xr[0])); o[1] = pk2(u0[2] + blo(xr[1]), u0[3] + bhi(xr[1]));
        o[2] = pk2(u1[0] + blo(xr[2]), u1[1] + bhi(xr[2])); o[3] = pk2(u1[2] + blo(xr[3]), u1[3] + bhi(xr[3]));
        __builtin_amdgcn_raw_buffer_store_b128(o, ro, h ? xo1 : xo0, xs, 0);
      }
    }
    STAMP(11);
  }
}

__global__ void dbg_kernel(char* ws) {
  const u64* s = (const u64*)(ws + (14u << 20));
  u64 t0 = ~0ull, tm = 0;
  for (int b = 0; b < 256; ++b) { t0 = s[b * 32] < t0 ? s[b * 32] : t0; tm = s[b * 32] > tm ? s[b * 32] : tm; }
  if (((t0 >> 3) & 127) != 0) return;
  float a[12];
#pragma unroll
  for (int i = 0; i < 12; ++i) { float v = 0; for (int b = 0; b < 256; ++b) if (((b >> 4) & 1) == (i == 2 || i == 3 ? 1 : 0)) v += (float)(s[b * 32 + i] - t0); a[i] = v * (0.01f / 128.f); }
  u64 e = 0; for (int b = 0; b < 256; ++b) e = s[b * 32 + 11] > e ? s[b * 32 + 11] : e;
  const u64* c = (const u64*)(ws + (15u << 20));
  printf("D s%.2f x%.2f kv%.2f g1 %.2f g2 %.2f w%.2f q%.2f p%.2f e%.2f max%.2f clk%.0f\n", (float)(tm - t0) * 0.01f, a[1], a[2], a[4], a[6], a[7], a[8], a[9], a[11], (float)(e - t0) * 0.01f,
         (float)(c[11] - c[0]) / (float)(s[11] - s[0]) * 100.f);
}
extern "C" void kernel_launch(void* const* d_in, const int* in_sizes, int n_in, void* d_out, int out_size, void* d_ws,
                              size_t ws_size, hipStream_t stream) {
  (void)in_sizes; (void)n_in; (void)out_size; (void)ws_size;
  (void)hipFuncSetAttribute((const void*)fused_kernel, hipFuncAttributeMaxDynamicSharedMemorySize, LDS_BYTES);
  fused_kernel<<<256, 512, LDS_BYTES, stream>>>(d_in[0], d_in[1], d_in[2], d_in[4], d_in[5], d_in[6], d_out, (char*)d_ws);
  dbg_kernel<<<1,1,0,stream>>>((char*)d_ws);
}
```

```cpp
#define DBG_STAMP
#define SI 0
#define SMAX 0
#define SK 0
#include <hip/hip_runtime.h>
#include <stdint.h>

typedef __attribute__((ext_vector_type(8))) __bf16 bf16x8;
typedef __attribute__((ext_vector_type(4))) __bf16 bf16x4;
typedef __attribute__((ext_vector_type(2))) __bf16 bf16x2;
typedef __attribute__((ext_vector_type(16))) float f32x16;
typedef __attribute__((ext_vector_type(4))) float f32x4;
typedef __attribute__((ext_vector_type(2))) float f32x2;
typedef __attribute__((ext_vector_type(4))) unsigned u32x4;
typedef __attribute__((ext_vector_type(2))) unsigned u32x2;
typedef __amdgpu_buffer_rsrc_t rsrc_t;
typedef unsigned long long u64;
#define AS3 __attribute__((address_space(3)))
#define AS1 __attribute__((address_space(1)))
#define DEV __device__ __forceinline__
#define SB0() __builtin_amdgcn_sched_barrier(0)

constexpr uint32_t SLOT = 20480, RING = 122880, MISC = RING, LDS_BYTES = 163840;
constexpr uint32_t WS_E = 0, WS_FT = 4096, WS_FA = 8192, WS_FB = 12288, WS_FM = 16384;
constexpr uint32_t WS_KVP = 32768, KVREC = 6912;
constexpr uint32_t WS_W2 = 1u << 20, WS_A2 = 4u << 20;
constexpr uint32_t WS_USED = WS_A2 + 128u * 68u * 1024u;
constexpr uint32_t SPIN_CAP = 1u << 16;

DEV rsrc_t mk_rsrc(const void* p, uint32_t bytes) {
  return __builtin_amdgcn_make_buffer_rsrc((void*)p, 0, bytes, 0x00020000);
}
DEV void dma16(uint32_t m0v, uint32_t voff, rsrc_t srd, uint32_t soff) {
  asm volatile("s_mov_b32 m0, %0\n\ts_nop 0\n\tbuffer_load_dwordx4 %1, %2, %3 offen lds"
               :: "s"(m0v), "v"(voff), "s"(srd), "s"(soff) : "memory");
}
DEV void dma16_sc1(uint32_t m0v, uint32_t voff, rsrc_t srd, uint32_t soff) {
  asm volatile("s_mov_b32 m0, %0\n\ts_nop 0\n\tbuffer_load_dwordx4 %1, %2, %3 offen sc1 lds"
               :: "s"(m0v), "v"(voff), "s"(srd), "s"(soff) : "memory");
}
DEV void dma16_if(uint32_t cond, uint32_t m0v, uint32_t voff, rsrc_t srd, uint32_t soff) {
  asm volatile("s_cmp_eq_u32 %4, 0\n\ts_cbranch_scc1 1f\n\ts_mov_b32 m0, %0\n\ts_nop 0\n\t"
               "buffer_load_dwordx4 %1, %2, %3 offen sc1 lds\n1:"
               :: "s"(m0v), "v"(voff), "s"(srd), "s"(soff), "s"(cond) : "memory", "scc");
}
template <int N> DEV void wait_vm() { asm volatile("s_waitcnt vmcnt(%0)" :: "n"(N) : "memory"); }
DEV void bar() { asm volatile("s_waitcnt lgkmcnt(0)\n\ts_barrier" ::: "memory"); }
template <int N> DEV void open_stage() {
  asm volatile("s_waitcnt vmcnt(%0) lgkmcnt(0)\n\ts_barrier" :: "n"(N) : "memory");
}
template <int N0, int N1> DEV void open_stage2(uint32_t sel) {
  asm volatile("s_cmp_eq_u32 %2, 0\n\ts_cbranch_scc1 1f\n\ts_waitcnt vmcnt(%1)\n1:\n\ts_waitcnt vmcnt(%0) lgkmcnt(0)\n\ts_barrier"
               :: "n"(N0), "n"(N1), "s"(sel) : "memory", "scc");
}
constexpr uint32_t PF = MISC + 40960 - 32;
struct Sync { uint32_t g; uint32_t pfr; u64 mask; };
template <int N0, int N1> DEV void arrive2(const Sync& y, uint32_t sel) {
  uint32_t t;
  asm volatile("s_cmp_eq_u32 %6, 0\n\ts_cbranch_scc1 1f\n\ts_waitcnt vmcnt(%5)\n1:\n\ts_waitcnt vmcnt(%4)\n\tv_mov_b32 %0, %1\n\t"
               "s_mov_b64 exec, %2\n\tds_write_b32 %3, %0\n\ts_mov_b64 exec, -1"
               : "=&v"(t) : "s"(y.g), "s"(y.mask), "v"(y.pfr), "n"(N0), "n"(N1), "s"(sel) : "memory", "scc");
}
template <int N> DEV void arrive(const Sync& y) {
  uint32_t t;
  asm volatile("s_waitcnt vmcnt(%4)\n\tv_mov_b32 %0, %1\n\ts_mov_b64 exec, %2\n\tds_write_b32 %3, %0\n\ts_mov_b64 exec, -1"
               : "=&v"(t) : "s"(y.g), "s"(y.mask), "v"(y.pfr), "n"(N) : "memory");
}
DEV uint32_t flagread(const Sync& y) {
  uint32_t f; asm volatile("ds_read_b32 %0, %1" : "=v"(f) : "v"(y.pfr) : "memory"); return f;
}
DEV void check(Sync& y, uint32_t f) {
  asm volatile("s_waitcnt lgkmcnt(0)\n\tv_cmp_gt_u32 vcc, %1, %0\n\ts_cbranch_vccz 2f\n1:\n\tds_read_b32 %0, %2\n\t"
               "s_waitcnt lgkmcnt(0)\n\tv_cmp_gt_u32 vcc, %1, %0\n\ts_cbranch_vccnz 1b\n2:"
               : "+v"(f) : "s"(y.g), "v"(y.pfr) : "memory", "vcc");
  y.g += 1;
}
DEV void publish_if(uint32_t cond, rsrc_t r, uint32_t soff, u64 val) {
  u64 t;
  asm volatile("s_cmp_eq_u32 %4, 0\n\ts_cbranch_scc1 1f\n\tv_mov_b64 %0, %1\n\ts_mov_b64 exec, 1\n\t"
               "buffer_store_dwordx2 %0, off, %2, %3 sc1\n\ts_mov_b64 exec, -1\n1:"
               : "=&v"(t) : "s"(val), "s"(r), "s"(soff), "s"(cond) : "memory", "scc");
}
typedef AS3 char* lptr;
DEV lptr lp(uint32_t a) { asm volatile("" : "+v"(a)); return (lptr)a; }
DEV bf16x4 trd(lptr a) { return __builtin_amdgcn_ds_read_tr16_b64_v4bf16((AS3 bf16x4*)a); }
DEV bf16x8 trd2(lptr a0, lptr a1) {
  bf16x4 lo = trd(a0), hi = trd(a1);
  return __builtin_shufflevector(lo, hi, 0, 1, 2, 3, 4, 5, 6, 7);
}
DEV u32x4 lds_r128(lptr a) { return *(AS3 u32x4*)a; }
DEV f32x4 lds_rf4(lptr a) { return *(AS3 f32x4*)a; }
DEV void lds_w128(lptr a, u32x4 v) { *(AS3 u32x4*)a = v; }
DEV float lds_rf(lptr a) { return *(AS3 float*)a; }
DEV void lds_wf(lptr a, float v) { *(AS3 float*)a = v; }
DEV int opq(int v) { asm volatile("" : "+v"(v)); return v; }
DEV uint32_t opqs(uint32_t v) { asm volatile("" : "+s"(v)); return v; }
DEV uint32_t pk2(float a, float b) {
  f32x2 v = {a, b};
  return __builtin_bit_cast(uint32_t, __builtin_convertvector(v, bf16x2));
}
DEV float blo(uint32_t u) { return __builtin_bit_cast(float, u << 16); }
DEV float bhi(uint32_t u) { return __builtin_bit_cast(float, u & 0xffff0000u); }
DEV float phi(float t) {
  float e = __builtin_amdgcn_exp2f(t * 1.4426950408889634f);
  e = __builtin_amdgcn_fmed3f(e, 0.f, 1.f);
  return fmaxf(t, 0.f) + e + 1e-6f;
}
#define MFMA32(a, b, c) __builtin_amdgcn_mfma_f32_32x32x16_bf16(a, b, c, 0, 0, 0)
#define MFMA16(a, b, c) __builtin_amdgcn_mfma_f32_16x16x32_bf16(a, b, c, 0, 0, 0)
DEV bf16x8 asbf(u32x4 v) { return __builtin_bit_cast(bf16x8, v); }
DEV u32x4 pack8(const f32x16& a, int s) {
  u32x4 r;
  r[0] = pk2(a[8 * s + 0], a[8 * s + 1]); r[1] = pk2(a[8 * s + 2], a[8 * s + 3]);
  r[2] = pk2(a[8 * s + 4], a[8 * s + 5]); r[3] = pk2(a[8 * s + 6], a[8 * s + 7]);
  return r;
}
DEV void st128_sc1(u32x4 v, rsrc_t r, uint32_t voff, uint32_t soff) {
  __builtin_amdgcn_raw_buffer_store_b128(v, r, voff, soff, 16);
}
DEV void flag_store(char* ws, uint32_t off, u64 v) {
  __hip_atomic_store((AS1 u64*)(ws + off), v, __ATOMIC_RELAXED, __HIP_MEMORY_SCOPE_AGENT);
}
DEV u64 flag_load(char* ws, uint32_t off) {
  return __hip_atomic_load((AS1 u64*)(ws + off), __ATOMIC_RELAXED, __HIP_MEMORY_SCOPE_AGENT);
}
DEV bool flag_ok(u64 v, u64 tag, uint32_t lvl) { return ((v ^ tag) >> 8) == 0 && (uint32_t)(v & 0xff) >= lvl; }
DEV u32x2 aload64(rsrc_t r, uint32_t voff, uint32_t soff) {
  u32x2 v; asm volatile("buffer_load_dwordx2 %0, %1, %2, %3 offen sc1" : "=v"(v) : "v"(voff), "s"(r), "s"(soff) : "memory"); return v;
}
DEV u32x4 aload128(rsrc_t r, uint32_t voff, uint32_t soff) {
  u32x4 v; asm volatile("buffer_load_dwordx4 %0, %1, %2, %3 offen sc1" : "=v"(v) : "v"(voff), "s"(r), "s"(soff) : "memory"); return v;
}
DEV u32x4 aload128p(rsrc_t r, uint32_t voff, uint32_t soff) {
  u32x4 v; asm volatile("buffer_load_dwordx4 %0, %1, %2, %3 offen" : "=v"(v) : "v"(voff), "s"(r), "s"(soff) : "memory"); return v;
}
template <typename T> DEV void fence_reg(T& v) { asm volatile("" : "+v"(v) :: "memory"); }
DEV u64 u64of(u32x2 v) { return ((u64)v[1] << 32) | v[0]; }
DEV void poll_wait(char* ws, uint32_t off, uint32_t lvl, u64 tag) {
  for (uint32_t it = 0; it < SPIN_CAP; ++it) {
    u64 v = flag_load(ws, off);
    if (__all(flag_ok(v, tag, lvl))) break;
    __builtin_amdgcn_s_sleep(1);
  }
}
DEV void check_or_wait(u32x2 got, char* ws, uint32_t off, uint32_t lvl, u64 tag) {
  if (!__all(flag_ok(u64of(got), tag, lvl))) poll_wait(ws, off, lvl, tag);
}
#define STAMP(i) do { if (tid == 0) { ((u64*)(ws + (14u << 20)))[blockIdx.x * 32 + (i)] = __builtin_amdgcn_s_memrealtime(); ((u64*)(ws + (15u << 20)))[blockIdx.x * 32 + (i)] = __builtin_amdgcn_s_memtime(); } } while (0)
#define NDOT 4

#define NORM1(R) "v_lshlrev_b32 %1, 16, v" #R "\n\tv_and_b32 v" #R ", 0xffff0000, v" #R "\n\tv_fma_f32 %1, %1, %2, %3\n\tv_fma_f32 v" #R ", v" #R ", %2, %3\n\tv_cvt_pk_bf16_f32 v" #R ", %1, v" #R "\n\t"
#define NORMX(I, A, B, C, D) case I: asm volatile("s_cmp_eq_u32 %4, 0\n\ts_cbranch_scc1 1f\n\t" NORM1(A) NORM1(B) NORM1(C) NORM1(D) "s_nop 0\n1:" \
    : "+{v[" #A ":" #D "]}"(f), "=&v"(t) : "v"(rs), "v"(nb), "s"(cond) : "scc"); break;
DEV void norm_frag_if(uint32_t cond, int i, u32x4& f, float rs, float nb) {
  float t;
  switch (i) {
    NORMX(0, 96, 97, 98, 99) NORMX(1, 100, 101, 102, 103) NORMX(2, 104, 105, 106, 107) NORMX(3, 108, 109, 110, 111)
    NORMX(4, 112, 113, 114, 115) NORMX(5, 116, 117, 118, 119) NORMX(6, 120, 121, 122, 123) NORMX(7, 124, 125, 126, 127)
    NORMX(8, 128, 129, 130, 131) NORMX(9, 132, 133, 134, 135) NORMX(10, 136, 137, 138, 139) NORMX(11, 140, 141, 142, 143)
    NORMX(12, 144, 145, 146, 147) NORMX(13, 148, 149, 150, 151) NORMX(14, 152, 153, 154, 155) NORMX(15, 156, 157, 158, 159)
    NORMX(16, 160, 161, 162, 163) NORMX(17, 164, 165, 166, 167) NORMX(18, 168, 169, 170, 171) NORMX(19, 172, 173, 174, 175)
    NORMX(20, 176, 177, 178, 179) NORMX(21, 180, 181, 182, 183) NORMX(22, 184, 185, 186, 187) NORMX(23, 188, 189, 190, 191)
    NORMX(24, 192, 193, 194, 195) NORMX(25, 196, 197, 198, 199) NORMX(26, 200, 201, 202, 203) NORMX(27, 204, 205, 206, 207)
    NORMX(28, 208, 209, 210, 211) NORMX(29, 212, 213, 214, 215) NORMX(30, 216, 217, 218, 219) NORMX(31, 220, 221, 222, 223)
    NORMX(32, 224, 225, 226, 227) NORMX(33, 228, 229, 230, 231) NORMX(34, 232, 233, 234, 235) NORMX(35, 236, 237, 238, 239)
    NORMX(36, 240, 241, 242, 243) NORMX(37, 244, 245, 246, 247) NORMX(38, 248, 249, 250, 251) NORMX(39, 252, 253, 254, 255)
  }
}
DEV uint32_t nxt(uint32_t s) { return s + SLOT == RING ? 0u : s + SLOT; }

DEV uint32_t kvq_voff(int piece, int lane, int i, bool isq) {
  uint32_t p = piece * 1024 + lane * 16;
  uint32_t row = (p * 52429u) >> 24;
  uint32_t col = p - row * 320;
  bool hi = col >= 160;
  uint32_t c = hi ? col - 160 : col;
  return isq ? (row + (hi ? 64u : 0u)) * 3840 + 160 * i + c
             : row * 3840 + (hi ? 2560u : 1280u) + 160 * i + c;
}
DEV void check4(u32x4 ft0, u32x4 ft1, char* ws, uint32_t off, u64 tag) {
  u32x2 f0 = {ft0[0], ft0[1]}, f1 = {ft0[2], ft0[3]}, f2 = {ft1[0], ft1[1]}, f3 = {ft1[2], ft1[3]};
  bool ok = flag_ok(u64of(f0), tag, 1) && flag_ok(u64of(f1), tag, 1) && flag_ok(u64of(f2), tag, 1) && flag_ok(u64of(f3), tag, 1);
  if (!__all(ok)) {
#pragma unroll 1
    for (int j = 0; j < 4; ++j) poll_wait(ws, off + 8 * j, 1, tag);
  }
}

template <int NH>
DEV void kv_chunk(u32x4 (&xf)[40], float rs, float nb, uint32_t& cur, uint32_t& prv, int tid, int w, int ci,
                  uint32_t kv0, uint32_t kv1, uint32_t kv2, uint32_t guvo, rsrc_t rqkv, rsrc_t rg_, rsrc_t ru_) {
  const int l = tid & 63;
  const uint32_t ta = (8 * (l >> 5) + ((l >> 2) & 3)) * 320 + ((l >> 4) & 1) * 32 + 8 * (l & 3) + NH * 192;
  f32x16 a0 = {}, a1 = {}, a2 = {};
  bf16x8 w0, w1, w2, n0, n1, n2;
  norm_frag_if(1, 0, xf[0], rs, nb);
  open_stage<8>();
  lptr base = lp(cur + ta);
  w0 = trd2(base, base + 1280); w1 = trd2(base + 64, base + 64 + 1280);
  if (NH == 0) w2 = trd2(base + 128, base + 128 + 1280);
#pragma unroll
  for (int s = 0; s < 10; ++s) {
#pragma unroll
    for (int t = 0; t < 4; ++t) {
      const int ks = 4 * s + t;
      SB0();
      if (s < 5) {
        if (t == 0) dma16(prv + w * 1024, kv0, rqkv, (s + 5) * 64 * 3840);
        if (t == 1) dma16(prv + (w + 8) * 1024, kv1, rqkv, (s + 5) * 64 * 3840);
        if (t == 2 && NH == 0) dma16(prv + (w + 16) * 1024, kv2, rqkv, (s + 5) * 64 * 3840);
      } else {
        if (t == 0) dma16(prv + w * 1024, guvo, rg_, (s - 5) * 64 * 3072 + (16 + ci) * 128);
        if (t == 1) dma16(prv + (w + 8) * 1024, guvo, ru_, (s - 5) * 64 * 3072 + (16 + ci) * 128);
      }
      if (t < 3 || s < 9) {
        if (t == 3) { open_stage<8>(); prv = cur; cur = nxt(cur); base = lp(cur + ta); }
        const lptr o = base + ((t + 1) & 3) * 5120;
        n0 = trd2(o, o + 1280); n1 = trd2(o + 64, o + 64 + 1280);
        if (NH == 0) n2 = trd2(o + 128, o + 128 + 1280);
      }
      SB0();
      a0 = MFMA32(asbf(xf[ks]), w0, a0);
      a1 = MFMA32(asbf(xf[ks]), w1, a1);
      if (NH == 0) a2 = MFMA32(asbf(xf[ks]), w2, a2);
      SB0();
      if (ks < 39) norm_frag_if(1, ks + 1, xf[ks + 1], rs, nb);
      w0 = n0; w1 = n1; if (NH == 0) w2 = n2;
    }
  }
  SB0();
  const int l2 = opq(tid) & 63;
  if (NH == 0) {
#pragma unroll
    for (int j = 0; j < 16; ++j) { a0[j] = phi(a0[j]); a1[j] = phi(a1[j]); float p = phi(a2[j]); a2[j] = (l2 & 16) ? a2[j] : p; }
  }
  const lptr e0 = lp(MISC + ((NH * 12 + (w & 3)) * 2) * 1024 + l2 * 16);
  lds_w128(e0, pack8(a0, 0)); lds_w128(e0 + 1024, pack8(a0, 1));
  lds_w128(e0 + 8192, pack8(a1, 0)); lds_w128(e0 + 8192 + 1024, pack8(a1, 1));
  if (NH == 0) { lds_w128(e0 + 16384, pack8(a2, 0)); lds_w128(e0 + 16384 + 1024, pack8(a2, 1)); }
}

template <int NH>
DEV void q_chunk(u32x4 (&xf)[40], uint32_t& cur, uint32_t& prv, int tid, int w, int ci, int rg, int ct, uint32_t fb_row,
                 rsrc_t rws, char* ws, u64 tag) {
  const int l = tid & 63, rb = w & 3;
  const uint32_t ta = (8 * (l >> 5) + ((l >> 2) & 3)) * 320 + ((l >> 4) & 1) * 32 + 8 * (l & 3) + NH * 128;
  const uint32_t ZP = MISC, ZS = MISC + 5120, X3 = MISC + 5632;
  const uint32_t l16 = l * 16;
  f32x16 a0 = {}, a1 = {};
  bf16x8 w0, w1, n0, n1;
  open_stage<8>();
  const int zt = min(tid, 319), zr = (zt * 3277) >> 16;
  u32x4 zp = aload128(rws, zr * KVREC + (zt - 20 * zr) * 16, WS_KVP + ci * 16 * KVREC + 6400);
  lptr base = lp(cur + ta);
  w0 = trd2(base, base + 1280);
  if (NH == 0) w1 = trd2(base + 64, base + 64 + 1280);
#pragma unroll
  for (int s = 0; s < 5; ++s) {
#pragma unroll
    for (int t = 0; t < 8; ++t) {
      const int ks = 8 * s + t;
      SB0();
      if (t < 2 || (t == 2 && NH == 0))
        dma16_sc1(prv + (w + 8 * t) * 1024, l16, rws, WS_W2 + (ct * 340 + s * 20 + w + 8 * t) * 1024);
      if (s == 2 && t == 0) {
        if (NH == 0 && tid == 0) flag_store(ws, WS_FB + rg * 128 + ci * 8, (tag & ~0xffull) | 2);
        fence_reg(zp); lds_w128(lp(ZP + zt * 16), zp);
      }
      if (NH == 1 && s == 3 && t == 0 && w < 6) {
        const int zi = min(tid - 256, 79);
        const lptr za = lp(ZP + zi * 4);
        float z = 0.f;
#pragma unroll
        for (int j = 0; j < 16; ++j) z += lds_rf(za + j * 320);
        lds_wf(za + 5120, z);
      }
      if (t < 7 || s < 4) {
        if (t == 7) {
          if (s == 1) open_stage<4>(); else open_stage<8>();
          prv = cur; cur = nxt(cur); base = lp(cur + ta);
        }
        const int tn = (t + 1) & 7;
        const lptr o = base + (tn & 3) * 5120 + (tn >> 2) * 160;
        n0 = trd2(o, o + 1280);
        if (NH == 0) n1 = trd2(o + 64, o + 64 + 1280);
      }
      SB0();
      a0 = MFMA32(w0, asbf(xf[ks]), a0);
      if (NH == 0) a1 = MFMA32(w1, asbf(xf[ks]), a1);
      w0 = n0; if (NH == 0) w1 = n1;
    }
  }
  SB0();
  prv = cur; cur = nxt(cur);
  STAMP(8);
  const int l2 = opq(tid) & 63, r = l2 & 31, hh = l2 >> 5;
  const uint32_t pv = (r >> 4) * (68 * 1024) + (hh * 16 + (r & 15)) * 16;
  const uint32_t qb = WS_A2 + ((rg * 8 + 2 * rb) * 68 + 48) * 1024 + ci * 5 * 512;
  const lptr zs = lp(ZS + 16 * hh), x3 = lp(X3 + (32 * rb + r) * 4);
  if (NH == 0) {
    u32x2 pl = aload64(rws, (l2 & 7) * 8, fb_row);
    float gs[8];
#pragma unroll
    for (int g = 0; g < 8; ++g) {
      const f32x4 z = lds_rf4(zs + 32 * g);
      float acc = 0.f;
#pragma unroll
      for (int j = 0; j < 4; ++j) {
        float v = phi(g < 4 ? a0[4 * g + j] : a1[4 * (g - 4) + j]);
        if (g < 4) a0[4 * g + j] = v; else a1[4 * (g - 4) + j] = v;
        acc = fmaf(v, z[j], acc);
      }
      gs[g] = acc;
    }
    float p0 = gs[0] + gs[1] + (hh ? 0.f : gs[2]);
    float p1 = gs[3] + gs[4] + (hh ? gs[2] : 0.f);
    float p2 = gs[5] + gs[6] + (hh ? 0.f : gs[7]);
    if (hh) lds_wf(x3, gs[7]);
    p0 += __shfl_xor(p0, 32); p1 += __shfl_xor(p1, 32); p2 += __shfl_xor(p2, 32);
    wait_vm<0>(); fence_reg(pl);
    if (w == 0) check_or_wait(pl, ws, fb_row + (l2 & 7) * 8, 2, tag);
    bar();
    const float p3 = gs[7] + lds_rf(x3 + 512);
    const float r0 = 1.f / (p0 + 1e-6f), r1 = 1.f / (p1 + 1e-6f), r2 = 1.f / (p2 + 1e-6f), r3 = 1.f / (p3 + 1e-6f);
    const float sc[8] = {r0, r0, hh ? r1 : r0, r1, r1, r2, r2, hh ? r3 : r2};
#pragma unroll
    for (int j = 0; j < 16; ++j) { a0[j] = a0[j] * sc[j >> 2]; a1[j] = a1[j] * sc[4 + (j >> 2)]; }
    st128_sc1(pack8(a0, 0), rws, pv, qb); st128_sc1(pack8(a0, 1), rws, pv, qb + 512);
    st128_sc1(pack8(a1, 0), rws, pv, qb + 1024); st128_sc1(pack8(a1, 1), rws, pv, qb + 1536);
  } else {
    float p3 = 0.f;
#pragma unroll
    for (int g = 0; g < 2; ++g) {
      const f32x4 z = lds_rf4(zs + 256 + 32 * g);
#pragma unroll
      for (int j = 0; j < 4; ++j) { float v = phi(a0[4 * g + j]); a0[4 * g + j] = v; p3 = fmaf(v, z[j], p3); }
    }
    p3 += __shfl_xor(p3, 32);
    if (hh) lds_wf(x3 + 512, p3);
    bar();
    p3 += lds_rf(x3);
    const float r3 = 1.f / (p3 + 1e-6f);
#pragma unroll
    for (int j = 0; j < 8; ++j) a0[j] = a0[j] * r3;
    st128_sc1(pack8(a0, 0), rws, pv, qb + 2048);
  }
}

__global__ __launch_bounds__(512, 2) void fused_kernel(const void* __restrict__ gx, const void* __restrict__ gwqkv,
                                                       const void* __restrict__ gwo, const void* __restrict__ gwg,
                                                       const void* __restrict__ gwu, const void* __restrict__ gwd,
                                                       void* __restrict__ gout, char* __restrict__ ws) {
  const int tid = threadIdx.x;
  const int w = __builtin_amdgcn_readfirstlane(tid >> 6);
  const int rb = w & 3, nh = w >> 2;
  const int b = blockIdx.x;
  const int xcd = b & 7, idx = b >> 3;
  const int rg = (xcd >> 2) * 8 + (idx >> 2);
  const bool iskv = (idx & 2) != 0;
  const int ci = 2 * (xcd & 3) + (idx & 1);
  const int tt = (xcd >> 1) * 8 + (idx >> 2), ct = (xcd & 1) * 4 + (idx & 3);

  const rsrc_t rx = mk_rsrc(gx, 2048 * 1280), rqkv = mk_rsrc(gwqkv, 640 * 3840);
  const rsrc_t rg_ = mk_rsrc(gwg, 640 * 3072), ru_ = mk_rsrc(gwu, 640 * 3072);
  const rsrc_t rws = mk_rsrc(ws, WS_USED);
  STAMP(0);

  uint32_t cur = 0, prv = 5 * SLOT;
  u64 tag;
  u32x4 xf[40];
  float rs, nb;
  uint32_t guvo;
  {
    const int l = tid & 63, hh = l >> 5, r = l & 31;
    u32x2 eld = aload64(rws, 0, WS_E);
    const int t0ks = b / 5, t0cg = b - 5 * t0ks;
    if (b < 240) dma16(MISC + w * 1024, (4 * w + (l >> 4)) * 1280 + (l & 15) * 16, mk_rsrc(gwd, 1536 * 1280), t0ks * 32 * 1280 + t0cg * 256);
    const uint32_t xvo = (8 * w + (l >> 3)) * 1280 + (((l & 7) ^ ((4 * w + (l >> 4)) & 7)) << 4);
    const uint32_t xso = rg * 128 * 1280;
#pragma unroll
    for (int s = 0; s < 5; ++s) {
      dma16(s * SLOT + w * 1024, xvo, rx, xso + s * 128);
      dma16(s * SLOT + (w + 8) * 1024, xvo, rx, xso + s * 128 + 64 * 1280);
    }
    guvo = (8 * w + (l >> 3)) * 3072 + (((l & 7) ^ (((l >> 4) & 1) << 2)) << 4);
    const uint32_t xra = (32 * rb + r) * 128 + ((hh ^ ((r >> 1) & 7)) << 4);
    float s0 = 0.f, s1 = 0.f, q0 = 0.f, q1 = 0.f;
    const uint32_t kv0 = iskv ? kvq_voff(w, l, ci, false) : guvo, kv1 = iskv ? kvq_voff(w + 8, l, ci, false) : guvo;
    const uint32_t kv2 = kvq_voff((w & 3) + 16, l, ci, false);
    const rsrc_t rf0 = iskv ? rqkv : rg_, rf1 = iskv ? rqkv : ru_;
    const uint32_t fstr = iskv ? 64 * 3840 : 64 * 3072, fso = iskv ? 0 : ci * 128, third = ((idx >> 1) & 1) & (nh ^ 1);
#pragma unroll
    for (int s = 0; s < 10; ++s) {
      open_stage<8>();
#pragma unroll
      for (int t = 0; t < 4; ++t) xf[4 * s + t] = lds_r128((lptr)(cur + (xra ^ (t << 5))));
      if (s < 5) {
        dma16(prv + w * 1024, xvo, rx, xso + (s + 5) * 128);
        dma16(prv + (w + 8) * 1024, xvo, rx, xso + (s + 5) * 128 + 64 * 1280);
      } else {
        dma16(prv + w * 1024, kv0, rf0, fso + (s - 5) * fstr);
        dma16(prv + (w + 8) * 1024, kv1, rf1, fso + (s - 5) * fstr);
        dma16_if(third, prv + (w + 16) * 1024, kv2, rqkv, (s - 5) * 64 * 3840);
      }
      if (s == 0) {
        fence_reg(eld);
        u64 e = ((u64)(uint32_t)__builtin_amdgcn_readfirstlane(eld[1]) << 32) | (uint32_t)__builtin_amdgcn_readfirstlane(eld[0]);
        tag = (e * 6364136223846793005ull + 1442695040888963407ull);
        if (b < 240) {
          const int g = l >> 4;
          const lptr a = lp(MISC + (16 * (g >> 1) + 4 * (g & 1) + ((l >> 2) & 3)) * 256 + w * 32 + 8 * (l & 3));
          bf16x8 f = trd2(a, a + 8 * 256);
          const int cb = 8 * t0cg + w, c5 = cb / 5, n5 = cb - 5 * c5;
          st128_sc1(__builtin_bit_cast(u32x4, f), rws, l * 16, WS_W2 + ((c5 * 68 + t0ks) * 5 + n5) * 1024);
        }
      }
      if (s == 6 && tid == 0) flag_store(ws, WS_FT + b * 8, (tag & ~0xffull) | 1);
#pragma unroll
      for (int t = 0; t < NDOT; ++t) {
        const uint32_t e0 = xf[4 * s + t][0], e1 = xf[4 * s + t][1], e2 = xf[4 * s + t][2], e3 = xf[4 * s + t][3];
        asm volatile("v_dot2c_f32_bf16 %0, %8, %4\n\tv_dot2c_f32_bf16 %2, %4, %4\n\tv_dot2c_f32_bf16 %1, %8, %5\n\tv_dot2c_f32_bf16 %3, %5, %5\n\t"
                     "v_dot2c_f32_bf16 %0, %8, %6\n\tv_dot2c_f32_bf16 %2, %6, %6\n\tv_dot2c_f32_bf16 %1, %8, %7\n\tv_dot2c_f32_bf16 %3, %7, %7"
                     : "+v"(s0), "+v"(s1), "+v"(q0), "+v"(q1) : "v"(e0), "v"(e1), "v"(e2), "v"(e3), "s"(0x3f803f80u));
      }
      SB0();
      prv = cur; cur = nxt(cur);
    }
    asm volatile("s_nop 3" : "+v"(s0), "+v"(s1), "+v"(q0), "+v"(q1));
    float sm = s0 + s1, sq = q0 + q1;
    sm += __shfl_xor(sm, 32); sq += __shfl_xor(sq, 32);
    const float mean = sm * (1.f / 640.f);
    const float var = fmaxf(sq * (1.f / 640.f) - mean * mean, 0.f);
    rs = 1.0f / sqrtf(var + 1e-5f);
    nb = -mean * rs;
    STAMP(1);

    if (iskv) {
      if (nh == 0) kv_chunk<0>(xf, rs, nb, cur, prv, opq(tid), w, ci, kv0, kv1, kv2, guvo, rqkv, rg_, ru_);
      if (opqs(nh) != 0) kv_chunk<1>(xf, rs, nb, cur, prv, opq(tid), w, ci, kv0, kv1, kv2, guvo, rqkv, rg_, ru_);
      STAMP(2);
      const int t2 = opq(tid), l = t2 & 63, hh = l >> 5, r = l & 31;
      u32x4 ft0, ft1;
      if (w == 7) { ft0 = aload128(rws, l * 32, WS_FT); ft1 = aload128(rws, l * 32, WS_FT + 16); }
      bar();
      const int kb = w < 5 ? (w >> 1) : (w - 5), vb = 2 + ((w + 1) >> 1);
      const lptr ka = lp(MISC + kb * 8192 + l * 16), va = lp(MISC + vb * 8192 + l * 16);
      f32x16 d = {};
      const uint32_t ck = cur;
      if (w < 5) {
#pragma unroll
        for (int j = 0; j < 8; ++j) d = MFMA32(asbf(lds_r128(ka + j * 1024)), asbf(lds_r128(va + j * 1024)), d);
        const int ve = 32 * vb + r - 80;
        const int hv = (ve * 205) >> 12;
        const int e = ve - 20 * hv;
        const int lo = 20 * hv - 32 * kb - 4 * hh;
        const uint32_t ca = ck + (hv * 400 + e - lo * 20) * 4;
#pragma unroll
        for (int j = 0; j < 16; ++j) {
          const int rowb = (j & 3) + 8 * (j >> 2);
          if (ve >= 0 && (unsigned)(rowb - lo) < 20u) lds_wf((lptr)(ca + rowb * 80), d[j]);
        }
      } else {
        const u32x4 ones = {0x3f803f80u, 0x3f803f80u, 0x3f803f80u, 0x3f803f80u};
#pragma unroll
        for (int j = 0; j < 8; ++j) d = MFMA32(asbf(ones), asbf(lds_r128(ka + j * 1024)), d);
        if (hh == 0 && 32 * kb + r < 80) lds_wf((lptr)(ck + 6400 + (32 * kb + r) * 4), d[0]);
      }
      if (w == 7) { wait_vm<0>(); fence_reg(ft0); fence_reg(ft1); check4(ft0, ft1, ws, WS_FT + l * 32, tag); }
      bar();
      if (t2 < 420) st128_sc1(lds_r128((lptr)(ck + t2 * 16)), rws, t2 * 16, WS_KVP + (ci * 16 + rg) * KVREC);
      prv = cur; cur = nxt(cur);
      STAMP(3);
    }
  }

  {
    const int ngu = iskv ? 1 : 2;
#pragma unroll 1
    for (int it = 0; it < ngu; ++it) {
      const int l = opq(tid) & 63;
      const uint32_t ga = (8 * (l >> 5) + ((l >> 2) & 3)) * 128 + 64 * (nh ^ ((l >> 3) & 1)) + 32 * ((l >> 4) & 1) + 8 * (l & 3);
      const uint32_t first = opqs((((idx >> 1) | it) & 1) ^ 1);
      const int c = iskv ? 16 + ci : it * 8 + ci;
      uint32_t nv0, nv1, nv2, nso, nstr; rsrc_t nr0, nr1;
      if (first) { nv0 = nv1 = nv2 = guvo; nr0 = rg_; nr1 = ru_; nso = (c + 8) * 128; nstr = 64 * 3072; }
      else if (!iskv) { nv0 = kvq_voff(w, l, ci, true); nv1 = kvq_voff(w + 8, l, ci, true); nv2 = kvq_voff((w & 3) + 16, l, ci, true);
                        nr0 = nr1 = rqkv; nso = 0; nstr = 128 * 3840; }
      else { nv0 = l * 16; nv1 = l * 16 + 8192; nv2 = l * 16 + 16384; nr0 = nr1 = rws; nso = WS_W2 + (ct * 340 + w) * 1024; nstr = 20480; }
      nso = __builtin_amdgcn_readfirstlane(nso); nstr = __builtin_amdgcn_readfirstlane(nstr);
      const uint32_t third = (first | nh) ^ 1;
      const uint32_t gso = opqs(c * 128);
      f32x16 ag = {}, au = {};
      bf16x8 w0, w1, n0, n1;
      norm_frag_if(first, 0, xf[0], rs, nb);
      open_stage<8>();
      lptr base = lp(cur + ga);
      w0 = trd2(base, base + 512); w1 = trd2(base + 8192, base + 8192 + 512);
#pragma unroll
      for (int s = 0; s < 10; ++s) {
#pragma unroll
        for (int t = 0; t < 4; ++t) {
          const int ks = 4 * s + t;
          SB0();
          if (s < 5) {
            if (t == 0) dma16(prv + w * 1024, guvo, rg_, (s + 5) * 64 * 3072 + gso);
            if (t == 1) dma16(prv + (w + 8) * 1024, guvo, ru_, (s + 5) * 64 * 3072 + gso);
          } else {
            if (t == 0) dma16_sc1(prv + w * 1024, nv0, nr0, nso);
            if (t == 1) dma16_sc1(prv + (w + 8) * 1024, nv1, nr1, nso);
            if (t == 2) { dma16_if(third, prv + (w + 16) * 1024, nv2, nr1, nso); nso += nstr; }
          }
          if (s == 4 && t == 0 && iskv && tid == 0) flag_store(ws, WS_FA + (ci * 16 + rg) * 8, (tag & ~0xffull) | 1);
          if (t < 3 || s < 9) {
            if (t == 3) {
 open_stage<8>();
 prv = cur; cur = nxt(cur); base = lp(cur + ga); }
            const lptr o = base + ((t + 1) & 3) * 2048;
            n0 = trd2(o, o + 512); n1 = trd2(o + 8192, o + 8192 + 512);
          }
          SB0();
          ag = MFMA32(w0, asbf(xf[ks]), ag);
          au = MFMA32(w1, asbf(xf[ks]), au);
          SB0();
          if (ks < 39) norm_frag_if(first, ks + 1, xf[ks + 1], rs, nb);
          w0 = n0; w1 = n1;
        }
      }
      SB0();
      prv = cur; cur = nxt(cur);
      STAMP(4 + 2 * it);
      const int l2 = opq(tid) & 63, r = l2 & 31, hh = l2 >> 5;
      const uint32_t fvo = first ? l2 * 32 : (l2 & 3) * 32, fso = first ? WS_FT : WS_FA + ci * 128;
      u32x4 ft0, ft1;
      if (w == 0) { ft0 = aload128(rws, fvo, fso); ft1 = aload128(rws, fvo, fso + 16); }
#pragma unroll
      for (int j = 0; j < 16; ++j) ag[j] = ag[j] * au[j];
      const uint32_t hv = (r >> 4) * (68 * 1024) + (hh * 16 + (r & 15)) * 16;
      const uint32_t hs = WS_A2 + ((rg * 8 + 2 * rb) * 68 + 2 * c + nh) * 1024;
      st128_sc1(pack8(ag, 0), rws, hv, hs);
      st128_sc1(pack8(ag, 1), rws, hv + 512, hs);
      if (w == 0) {
        wait_vm<2>(); fence_reg(ft0); fence_reg(ft1);
        check4(ft0, ft1, ws, fso + fvo, tag);
        if (b == 0 && first && l2 == 0) flag_store(ws, WS_E, tag);
      }
      STAMP(5 + 2 * it);
    }
  }

  uint32_t pub_off, pub_lvl;
  const uint32_t fb_row = WS_FB + (tt >> 1) * 128;
  if (!iskv) {
    if (nh == 0) q_chunk<0>(xf, cur, prv, opq(tid), w, ci, rg, ct, fb_row, rws, ws, tag);
    else q_chunk<1>(xf, cur, prv, opq(tid), w, ci, rg, ct, fb_row, rws, ws, tag);
    pub_off = WS_FB + rg * 128 + ci * 8; pub_lvl = 3;
  } else {
    const int hl = rg & 3, qd = rg >> 2, head = 4 * ci + hl;
    const int tid = opq(threadIdx.x), l = tid & 63, q4 = (l >> 2) & 3, p4 = l & 3;
    const uint32_t WO = MISC, RED = MISC + 6400;
    bar();
    u32x2 pl; u32x4 wo4, k0, k1, k2, k3;
    if (w == 0) pl = aload64(rws, (l & 7) * 8, fb_row);
    if (tid < 400) {
      const int row = (tid * 3277) >> 16, rq = (tid * 656) >> 16;
      wo4 = aload128p(mk_rsrc(gwo, 640 * 1280), row * 1280 + (tid - 20 * row) * 16, head * 20 * 1280 + qd * 320);
      const uint32_t vo = rq * 4 * KVREC + (tid - 100 * rq) * 16, so = WS_KVP + ci * 16 * KVREC + hl * 1600;
      k0 = aload128(rws, vo, so); k1 = aload128(rws, vo, so + KVREC);
      k2 = aload128(rws, vo, so + 2 * KVREC); k3 = aload128(rws, vo, so + 3 * KVREC);
    }
    wait_vm<0>();
    if (tid < 400) {
      fence_reg(wo4); fence_reg(k0); fence_reg(k1); fence_reg(k2); fence_reg(k3);
      lds_w128((lptr)(WO + tid * 16), wo4);
      f32x4 sum = (__builtin_bit_cast(f32x4, k0) + __builtin_bit_cast(f32x4, k1)) + (__builtin_bit_cast(f32x4, k2) + __builtin_bit_cast(f32x4, k3));
      lds_w128((lptr)(RED + tid * 16), __builtin_bit_cast(u32x4, sum));
    }
    if (w == 0) { fence_reg(pl); check_or_wait(pl, ws, fb_row + (l & 7) * 8, 2, tag); }
    bar();
    if (tid == 0) flag_store(ws, WS_FB + rg * 128 + (8 + ci) * 8, (tag & ~0xffull) | 1);
    STAMP(8);
    {
      const int i16 = l & 15, g = l >> 4;
      u32x4 af[2];
#pragma unroll
      for (int db = 0; db < 2; ++db) {
        const int d = min(16 * db + i16, 19);
        f32x4 lo = {}, hi = {};
        const lptr al = lp(RED + d * 80 + min(g, 2) * 32), ah = lp(RED + d * 80 + min(g, 2) * 32 + (g < 2 ? 16 : 0));
#pragma unroll
        for (int j = 0; j < 4; ++j) {
          lo += lds_rf4(al + j * 1600);
          hi += lds_rf4(ah + j * 1600);
        }
        af[db][0] = g < 3 ? pk2(lo[0], lo[1]) : 0u; af[db][1] = g < 3 ? pk2(lo[2], lo[3]) : 0u;
        af[db][2] = g < 2 ? pk2(hi[0], hi[1]) : 0u; af[db][3] = g < 2 ? pk2(hi[2], hi[3]) : 0u;
      }
      const uint32_t b0 = WO + min(8 * g + q4, 16 + q4) * 320 + 8 * p4, b1 = WO + min(8 * g + 4 + q4, 16 + q4) * 320 + 8 * p4;
      for (int n = w; n < 10; n += 8) {
        const bf16x8 bf = trd2((lptr)(b0 + n * 32), (lptr)(b1 + n * 32));
        const f32x4 z4 = {};
        const f32x4 m0 = MFMA16(asbf(af[0]), bf, z4), m1 = MFMA16(asbf(af[1]), bf, z4);
        const int cb = 10 * qd + n, c5 = cb / 5, n5 = cb - 5 * c5;
        const uint32_t fb = WS_W2 + (c5 * 340 + 240 + n5) * 1024 + i16 * 16;
        const int G0 = 5 * head + g, G1 = 5 * head + 4;
        const uint32_t o0 = fb + (G0 >> 3) * 5120 + (((G0 >> 2) & 1) * 2 + (G0 & 1)) * 256 + ((G0 >> 1) & 1) * 8;
        const uint32_t o1 = fb + (G1 >> 3) * 5120 + (((G1 >> 2) & 1) * 2 + (G1 & 1)) * 256 + ((G1 >> 1) & 1) * 8;
        u32x2 v0 = {pk2(m0[0], m0[1]), pk2(m0[2], m0[3])}, v1 = {pk2(m1[0], m1[1]), pk2(m1[2], m1[3])};
        __builtin_amdgcn_raw_buffer_store_b64(v0, rws, o0, 0, 16);
        if (g == 0) __builtin_amdgcn_raw_buffer_store_b64(v1, rws, o1, 0, 16);
      }
    }
    pub_off = WS_FM + (qd * 32 + head) * 8; pub_lvl = 1;
  }
  STAMP(9);

  {
    const int kh = nh;
    const int tid = opq(threadIdx.x), l = tid & 63;
    u32x4 A[34];
    const int pr0 = (tid * 6554) >> 16;
    const int pr1 = ((tid + 512) * 6554) >> 16;
    const uint32_t xo0 = pr0 * 1280 + (tid - 10 * pr0) * 16, xo1 = pr1 * 1280 + (tid + 512 - 10 * pr1) * 16;
    const uint32_t xs = tt * 64 * 1280 + ct * 160;
    u32x4 xr0 = aload128p(rx, xo0, xs), xr1;
    if (tid < 128) xr1 = aload128p(rx, xo1, xs);
    const uint32_t as = WS_A2 + ((tt * 4 + rb) * 68 + 2 * kh) * 1024;
    const uint32_t l16 = l * 16;
#pragma unroll
    for (int p = 0; p < 5; ++p) { A[2 * p] = aload128(rws, l16, as + p * 4096); A[2 * p + 1] = aload128(rws, l16, as + p * 4096 + 1024); }
    u32x2 pl;
    const uint32_t po2 = fb_row + 64 + (l & 7) * 8;
    const uint32_t po3 = l < 32 ? WS_FM + (ct >> 1) * 256 + l * 8 : fb_row + (l & 7) * 8;
    if (w == 0) pl = aload64(rws, po2, 0);
    f32x4 acc[5] = {};
#pragma unroll
    for (int p = 0; p < 17; ++p) {
      const int lo = p < 5 ? 0 : p - 4, hi = p - 1 < 11 ? p - 1 : 11;
      const int nop = hi >= lo ? hi - lo + 1 : 0, ex = p < 5 ? 2 * (4 - p) : 0;
      if (p == 3 || p == 7) {
        if (w == 0) {
          wait_vm<15>(); fence_reg(pl);
          if (p == 3) check_or_wait(pl, ws, po2, 1, tag); else check_or_wait(pl, ws, po3, l < 32 ? 1 : 3, tag);
        }
      }
      switch (nop * 16 + ex) {
#define OPEN_CASE(NOP, EX) case NOP * 16 + EX: open_stage2<NOP * 5 + EX, NOP * 4 + EX>(nh); break;
        OPEN_CASE(0, 8) OPEN_CASE(1, 6) OPEN_CASE(2, 4) OPEN_CASE(3, 2) OPEN_CASE(4, 0)
        OPEN_CASE(3, 0) OPEN_CASE(2, 0) OPEN_CASE(1, 0) OPEN_CASE(0, 0)
        default: open_stage<0>();
      }
      fence_reg(A[2 * p]); fence_reg(A[2 * p + 1]);
      if (p == 0 && tid == 0) flag_store(ws, pub_off, (tag & ~0xffull) | pub_lvl);
      const lptr rbs = lp(cur + kh * 10240 + l16);
      u32x4 B[10];
#pragma unroll
      for (int j = 0; j < 10; ++j) B[j] = lds_r128(rbs + j * 1024);
      SB0();
      if (p + 5 < 17) {
        const uint32_t so = WS_W2 + (ct * 340 + (p + 5) * 20 + w) * 1024;
        dma16_sc1(prv + w * 1024, l16, rws, so);
        dma16_sc1(prv + (w + 8) * 1024, l16, rws, so + 8192);
        dma16_if(nh ^ 1, prv + (w + 16) * 1024, l16, rws, so + 16384);
        A[2 * p + 10] = aload128(rws, l16, as + (p + 5) * 4096);
        A[2 * p + 11] = aload128(rws, l16, as + (p + 5) * 4096 + 1024);
      }
      if (p == 3 && w == 0) pl = aload64(rws, po3, 0);
      SB0();
#pragma unroll
      for (int j = 0; j < 10; ++j) acc[j % 5] = MFMA16(asbf(B[j]), asbf(A[2 * p + j / 5]), acc[j % 5]);
      SB0();
      prv = cur; cur = nxt(cur);
    }
    STAMP(10);
    bar();
    const lptr ow = lp((kh * 64 + rb * 16 + (l & 15)) * 336 + (l >> 4) * 16);
#pragma unroll
    for (int j = 0; j < 5; ++j) lds_w128(ow + j * 64, __builtin_bit_cast(u32x4, acc[j]));
    bar();
    const rsrc_t ro = mk_rsrc(gout, 2048 * 1280);
#pragma unroll
    for (int h = 0; h < 2; ++h) {
      if (h == 0 || tid < 128) {
        u32x4 xr = h ? xr1 : xr0;
        fence_reg(xr);
        const int pr = h ? pr1 : pr0, pc = tid + 512 * h - 10 * pr;
        const lptr a = lp(pr * 336 + pc * 32);
        f32x4 u0 = lds_rf4(a) + lds_rf4(a + 64 * 336);
        f32x4 u1 = lds_rf4(a + 16) + lds_rf4(a + 16 + 64 * 336);
        u32x4 o;
        o[0] = pk2(u0[0] + blo(xr[0]), u0[1] + bhi(xr[0])); o[1] = pk2(u0[2] + blo(xr[1]), u0[3] + bhi(xr[1]));
        o[2] = pk2(u1[0] + blo(xr[2]), u1[1] + bhi(xr[2])); o[3] = pk2(u1[2] + blo(xr[3]), u1[3] + bhi(xr[3]));
        __builtin_amdgcn_raw_buffer_store_b128(o, ro, h ? xo1 : xo0, xs, 0);
      }
    }
    STAMP(11);
  }
}

__global__ void dbg_kernel(char* ws) {
  const u64* s = (const u64*)(ws + (14u << 20));
  const int l = threadIdx.x;
  u64 t0 = ~0ull; float v = 0.f;
  for (int j = 0; j < 4; ++j) { u64 a = s[(l * 4 + j) * 32]; t0 = a < t0 ? a : t0; }
  for (int o = 1; o < 64; o <<= 1) { u64 a = __shfl_xor(t0, o); t0 = a < t0 ? a : t0; }
  for (int j = 0; j < 4; ++j) { int b = l * 4 + j; float d = (float)(s[b * 32 + SI] - t0); if (SMAX) v = fmaxf(v, d); else if (((b >> 4) & 1) == 0) v += d * (1.f / 128.f); }
  for (int o = 1; o < 64; o <<= 1) { float a = __shfl_xor(v, o); v = SMAX ? fmaxf(v, a) : v + a; }
  u64 n = (u64)(v * SK); if (n > 20000) n = 20000;
  u64 st = __builtin_amdgcn_s_memrealtime();
  while (__builtin_amdgcn_s_memrealtime() - st < n) {}
}
extern "C" void kernel_launch(void* const* d_in, const int* in_sizes, int n_in, void* d_out, int out_size, void* d_ws,
                              size_t ws_size, hipStream_t stream) {
  (void)in_sizes; (void)n_in; (void)out_size; (void)ws_size;
  (void)hipFuncSetAttribute((const void*)fused_kernel, hipFuncAttributeMaxDynamicSharedMemorySize, LDS_BYTES);
  fused_kernel<<<256, 512, LDS_BYTES, stream>>>(d_in[0], d_in[1], d_in[2], d_in[4], d_in[5], d_in[6], d_out, (char*)d_ws);
  dbg_kernel<<<1,64,0,stream>>>((char*)d_ws);
}
```

```cpp
#define DBG_STAMP
#define SI 0
#include <hip/hip_runtime.h>
#include <stdint.h>

typedef __attribute__((ext_vector_type(8))) __bf16 bf16x8;
typedef __attribute__((ext_vector_type(4))) __bf16 bf16x4;
typedef __attribute__((ext_vector_type(2))) __bf16 bf16x2;
typedef __attribute__((ext_vector_type(16))) float f32x16;
typedef __attribute__((ext_vector_type(4))) float f32x4;
typedef __attribute__((ext_vector_type(2))) float f32x2;
typedef __attribute__((ext_vector_type(4))) unsigned u32x4;
typedef __attribute__((ext_vector_type(2))) unsigned u32x2;
typedef __amdgpu_buffer_rsrc_t rsrc_t;
typedef unsigned long long u64;
#define AS3 __attribute__((address_space(3)))
#define AS1 __attribute__((address_space(1)))
#define DEV __device__ __forceinline__
#define SB0() __builtin_amdgcn_sched_barrier(0)

constexpr uint32_t SLOT = 20480, RING = 122880, MISC = RING, LDS_BYTES = 163840;
constexpr uint32_t WS_E = 0, WS_FT = 4096, WS_FA = 8192, WS_FB = 12288, WS_FM = 16384;
constexpr uint32_t WS_KVP = 32768, KVREC = 6912;
constexpr uint32_t WS_W2 = 1u << 20, WS_A2 = 4u << 20;
constexpr uint32_t WS_USED = WS_A2 + 128u * 68u * 1024u;
constexpr uint32_t SPIN_CAP = 1u << 16;

DEV rsrc_t mk_rsrc(const void* p, uint32_t bytes) {
  return __builtin_amdgcn_make_buffer_rsrc((void*)p, 0, bytes, 0x00020000);
}
DEV void dma16(uint32_t m0v, uint32_t voff, rsrc_t srd, uint32_t soff) {
  asm volatile("s_mov_b32 m0, %0\n\ts_nop 0\n\tbuffer_load_dwordx4 %1, %2, %3 offen lds"
               :: "s"(m0v), "v"(voff), "s"(srd), "s"(soff) : "memory");
}
DEV void dma16_sc1(uint32_t m0v, uint32_t voff, rsrc_t srd, uint32_t soff) {
  asm volatile("s_mov_b32 m0, %0\n\ts_nop 0\n\tbuffer_load_dwordx4 %1, %2, %3 offen sc1 lds"
               :: "s"(m0v), "v"(voff), "s"(srd), "s"(soff) : "memory");
}
DEV void dma16_if(uint32_t cond, uint32_t m0v, uint32_t voff, rsrc_t srd, uint32_t soff) {
  asm volatile("s_cmp_eq_u32 %4, 0\n\ts_cbranch_scc1 1f\n\ts_mov_b32 m0, %0\n\ts_nop 0\n\t"
               "buffer_load_dwordx4 %1, %2, %3 offen sc1 lds\n1:"
               :: "s"(m0v), "v"(voff), "s"(srd), "s"(soff), "s"(cond) : "memory", "scc");
}
template <int N> DEV void wait_vm() { asm volatile("s_waitcnt vmcnt(%0)" :: "n"(N) : "memory"); }
DEV void bar() { asm volatile("s_waitcnt lgkmcnt(0)\n\ts_barrier" ::: "memory"); }
template <int N> DEV void open_stage() {
  asm volatile("s_waitcnt vmcnt(%0) lgkmcnt(0)\n\ts_barrier" :: "n"(N) : "memory");
}
template <int N0, int N1> DEV void open_stage2(uint32_t sel) {
  asm volatile("s_cmp_eq_u32 %2, 0\n\ts_cbranch_scc1 1f\n\ts_waitcnt vmcnt(%1)\n1:\n\ts_waitcnt vmcnt(%0) lgkmcnt(0)\n\ts_barrier"
               :: "n"(N0), "n"(N1), "s"(sel) : "memory", "scc");
}
constexpr uint32_t PF = MISC + 40960 - 32;
struct Sync { uint32_t g; uint32_t pfr; u64 mask; };
template <int N0, int N1> DEV void arrive2(const Sync& y, uint32_t sel) {
  uint32_t t;
  asm volatile("s_cmp_eq_u32 %6, 0\n\ts_cbranch_scc1 1f\n\ts_waitcnt vmcnt(%5)\n1:\n\ts_waitcnt vmcnt(%4)\n\tv_mov_b32 %0, %1\n\t"
               "s_mov_b64 exec, %2\n\tds_write_b32 %3, %0\n\ts_mov_b64 exec, -1"
               : "=&v"(t) : "s"(y.g), "s"(y.mask), "v"(y.pfr), "n"(N0), "n"(N1), "s"(sel) : "memory", "scc");
}
template <int N> DEV void arrive(const Sync& y) {
  uint32_t t;
  asm volatile("s_waitcnt vmcnt(%4)\n\tv_mov_b32 %0, %1\n\ts_mov_b64 exec, %2\n\tds_write_b32 %3, %0\n\ts_mov_b64 exec, -1"
               : "=&v"(t) : "s"(y.g), "s"(y.mask), "v"(y.pfr), "n"(N) : "memory");
}
DEV uint32_t flagread(const Sync& y) {
  uint32_t f; asm volatile("ds_read_b32 %0, %1" : "=v"(f) : "v"(y.pfr) : "memory"); return f;
}
DEV void check(Sync& y, uint32_t f) {
  asm volatile("s_waitcnt lgkmcnt(0)\n\tv_cmp_gt_u32 vcc, %1, %0\n\ts_cbranch_vccz 2f\n1:\n\tds_read_b32 %0, %2\n\t"
               "s_waitcnt lgkmcnt(0)\n\tv_cmp_gt_u32 vcc, %1, %0\n\ts_cbranch_vccnz 1b\n2:"
               : "+v"(f) : "s"(y.g), "v"(y.pfr) : "memory", "vcc");
  y.g += 1;
}
DEV void publish_if(uint32_t cond, rsrc_t r, uint32_t soff, u64 val) {
  u64 t;
  asm volatile("s_cmp_eq_u32 %4, 0\n\ts_cbranch_scc1 1f\n\tv_mov_b64 %0, %1\n\ts_mov_b64 exec, 1\n\t"
               "buffer_store_dwordx2 %0, off, %2, %3 sc1\n\ts_mov_b64 exec, -1\n1:"
               : "=&v"(t) : "s"(val), "s"(r), "s"(soff), "s"(cond) : "memory", "scc");
}
typedef AS3 char* lptr;
DEV lptr lp(uint32_t a) { asm volatile("" : "+v"(a)); return (lptr)a; }
DEV bf16x4 trd(lptr a) { return __builtin_amdgcn_ds_read_tr16_b64_v4bf16((AS3 bf16x4*)a); }
DEV bf16x8 trd2(lptr a0, lptr a1) {
  bf16x4 lo = trd(a0), hi = trd(a1);
  return __builtin_shufflevector(lo, hi, 0, 1, 2, 3, 4, 5, 6, 7);
}
DEV u32x4 lds_r128(lptr a) { return *(AS3 u32x4*)a; }
DEV f32x4 lds_rf4(lptr a) { return *(AS3 f32x4*)a; }
DEV void lds_w128(lptr a, u32x4 v) { *(AS3 u32x4*)a = v; }
DEV float lds_rf(lptr a) { return *(AS3 float*)a; }
DEV void lds_wf(lptr a, float v) { *(AS3 float*)a = v; }
DEV int opq(int v) { asm volatile("" : "+v"(v)); return v; }
DEV uint32_t opqs(uint32_t v) { asm volatile("" : "+s"(v)); return v; }
DEV uint32_t pk2(float a, float b) {
  f32x2 v = {a, b};
  return __builtin_bit_cast(uint32_t, __builtin_convertvector(v, bf16x2));
}
DEV float blo(uint32_t u) { return __builtin_bit_cast(float, u << 16); }
DEV float bhi(uint32_t u) { return __builtin_bit_cast(float, u & 0xffff0000u); }
DEV float phi(float t) {
  float e = __builtin_amdgcn_exp2f(t * 1.4426950408889634f);
  e = __builtin_amdgcn_fmed3f(e, 0.f, 1.f);
  return fmaxf(t, 0.f) + e + 1e-6f;
}
#define MFMA32(a, b, c) __builtin_amdgcn_mfma_f32_32x32x16_bf16(a, b, c, 0, 0, 0)
#define MFMA16(a, b, c) __builtin_amdgcn_mfma_f32_16x16x32_bf16(a, b, c, 0, 0, 0)
DEV bf16x8 asbf(u32x4 v) { return __builtin_bit_cast(bf16x8, v); }
DEV u32x4 pack8(const f32x16& a, int s) {
  u32x4 r;
  r[0] = pk2(a[8 * s + 0], a[8 * s + 1]); r[1] = pk2(a[8 * s + 2], a[8 * s + 3]);
  r[2] = pk2(a[8 * s + 4], a[8 * s + 5]); r[3] = pk2(a[8 * s + 6], a[8 * s + 7]);
  return r;
}
DEV void st128_sc1(u32x4 v, rsrc_t r, uint32_t voff, uint32_t soff) {
  __builtin_amdgcn_raw_buffer_store_b128(v, r, voff, soff, 16);
}
DEV void flag_store(char* ws, uint32_t off, u64 v) {
  __hip_atomic_store((AS1 u64*)(ws + off), v, __ATOMIC_RELAXED, __HIP_MEMORY_SCOPE_AGENT);
}
DEV u64 flag_load(char* ws, uint32_t off) {
  return __hip_atomic_load((AS1 u64*)(ws + off), __ATOMIC_RELAXED, __HIP_MEMORY_SCOPE_AGENT);
}
DEV bool flag_ok(u64 v, u64 tag, uint32_t lvl) { return ((v ^ tag) >> 8) == 0 && (uint32_t)(v & 0xff) >= lvl; }
DEV u32x2 aload64(rsrc_t r, uint32_t voff, uint32_t soff) {
  u32x2 v; asm volatile("buffer_load_dwordx2 %0, %1, %2, %3 offen sc1" : "=v"(v) : "v"(voff), "s"(r), "s"(soff) : "memory"); return v;
}
DEV u32x4 aload128(rsrc_t r, uint32_t voff, uint32_t soff) {
  u32x4 v; asm volatile("buffer_load_dwordx4 %0, %1, %2, %3 offen sc1" : "=v"(v) : "v"(voff), "s"(r), "s"(soff) : "memory"); return v;
}
DEV u32x4 aload128p(rsrc_t r, uint32_t voff, uint32_t soff) {
  u32x4 v; asm volatile("buffer_load_dwordx4 %0, %1, %2, %3 offen" : "=v"(v) : "v"(voff), "s"(r), "s"(soff) : "memory"); return v;
}
template <typename T> DEV void fence_reg(T& v) { asm volatile("" : "+v"(v) :: "memory"); }
DEV u64 u64of(u32x2 v) { return ((u64)v[1] << 32) | v[0]; }
DEV void poll_wait(char* ws, uint32_t off, uint32_t lvl, u64 tag) {
  for (uint32_t it = 0; it < SPIN_CAP; ++it) {
    u64 v = flag_load(ws, off);
    if (__all(flag_ok(v, tag, lvl))) break;
    __builtin_amdgcn_s_sleep(1);
  }
}
DEV void check_or_wait(u32x2 got, char* ws, uint32_t off, uint32_t lvl, u64 tag) {
  if (!__all(flag_ok(u64of(got), tag, lvl))) poll_wait(ws, off, lvl, tag);
}
#define STAMP(i) do { if (tid == 0) { ((u64*)(ws + (14u << 20)))[blockIdx.x * 32 + (i)] = __builtin_amdgcn_s_memrealtime(); ((u64*)(ws + (15u << 20)))[blockIdx.x * 32 + (i)] = __builtin_amdgcn_s_memtime(); } } while (0)
#define NDOT 4

#define NORM1(R) "v_lshlrev_b32 %1, 16, v" #R "\n\tv_and_b32 v" #R ", 0xffff0000, v" #R "\n\tv_fma_f32 %1, %1, %2, %3\n\tv_fma_f32 v" #R ", v" #R ", %2, %3\n\tv_cvt_pk_bf16_f32 v" #R ", %1, v" #R "\n\t"
#define NORMX(I, A, B, C, D) case I: asm volatile("s_cmp_eq_u32 %4, 0\n\ts_cbranch_scc1 1f\n\t" NORM1(A) NORM1(B) NORM1(C) NORM1(D) "s_nop 0\n1:" \
    : "+{v[" #A ":" #D "]}"(f), "=&v"(t) : "v"(rs), "v"(nb), "s"(cond) : "scc"); break;
DEV void norm_frag_if(uint32_t cond, int i, u32x4& f, float rs, float nb) {
  float t;
  switch (i) {
    NORMX(0, 96, 97, 98, 99) NORMX(1, 100, 101, 102, 103) NORMX(2, 104, 105, 106, 107) NORMX(3, 108, 109, 110, 111)
    NORMX(4, 112, 113, 114, 115) NORMX(5, 116, 117, 118, 119) NORMX(6, 120, 121, 122, 123) NORMX(7, 124, 125, 126, 127)
    NORMX(8, 128, 129, 130, 131) NORMX(9, 132, 133, 134, 135) NORMX(10, 136, 137, 138, 139) NORMX(11, 140, 141, 142, 143)
    NORMX(12, 144, 145, 146, 147) NORMX(13, 148, 149, 150, 151) NORMX(14, 152, 153, 154, 155) NORMX(15, 156, 157, 158, 159)
    NORMX(16, 160, 161, 162, 163) NORMX(17, 164, 165, 166, 167) NORMX(18, 168, 169, 170, 171) NORMX(19, 172, 173, 174, 175)
    NORMX(20, 176, 177, 178, 179) NORMX(21, 180, 181, 182, 183) NORMX(22, 184, 185, 186, 187) NORMX(23, 188, 189, 190, 191)
    NORMX(24, 192, 193, 194, 195) NORMX(25, 196, 197, 198, 199) NORMX(26, 200, 201, 202, 203) NORMX(27, 204, 205, 206, 207)
    NORMX(28, 208, 209, 210, 211) NORMX(29, 212, 213, 214, 215) NORMX(30, 216, 217, 218, 219) NORMX(31, 220, 221, 222, 223)
    NORMX(32, 224, 225, 226, 227) NORMX(33, 228, 229, 230, 231) NORMX(34, 232, 233, 234, 235) NORMX(35, 236, 237, 238, 239)
    NORMX(36, 240, 241, 242, 243) NORMX(37, 244, 245, 246, 247) NORMX(38, 248, 249, 250, 251) NORMX(39, 252, 253, 254, 255)
  }
}
DEV uint32_t nxt(uint32_t s) { return s + SLOT == RING ? 0u : s + SLOT; }

DEV uint32_t kvq_voff(int piece, int lane, int i, bool isq) {
  uint32_t p = piece * 1024 + lane * 16;
  uint32_t row = (p * 52429u) >> 24;
  uint32_t col = p - row * 320;
  bool hi = col >= 160;
  uint32_t c = hi ? col - 160 : col;
  return isq ? (row + (hi ? 64u : 0u)) * 3840 + 160 * i + c
             : row * 3840 + (hi ? 2560u : 1280u) + 160 * i + c;
}
DEV void check4(u32x4 ft0, u32x4 ft1, char* ws, uint32_t off, u64 tag) {
  u32x2 f0 = {ft0[0], ft0[1]}, f1 = {ft0[2], ft0[3]}, f2 = {ft1[0], ft1[1]}, f3 = {ft1[2], ft1[3]};
  bool ok = flag_ok(u64of(f0), tag, 1) && flag_ok(u64of(f1), tag, 1) && flag_ok(u64of(f2), tag, 1) && flag_ok(u64of(f3), tag, 1);
  if (!__all(ok)) {
#pragma unroll 1
    for (int j = 0; j < 4; ++j) poll_wait(ws, off + 8 * j, 1, tag);
  }
}

template <int NH>
DEV void kv_chunk(u32x4 (&xf)[40], float rs, float nb, uint32_t& cur, uint32_t& prv, int tid, int w, int ci,
                  uint32_t kv0, uint32_t kv1, uint32_t kv2, uint32_t guvo, rsrc_t rqkv, rsrc_t rg_, rsrc_t ru_) {
  const int l = tid & 63;
  const uint32_t ta = (8 * (l >> 5) + ((l >> 2) & 3)) * 320 + ((l >> 4) & 1) * 32 + 8 * (l & 3) + NH * 192;
  f32x16 a0 = {}, a1 = {}, a2 = {};
  bf16x8 w0, w1, w2, n0, n1, n2;
  norm_frag_if(1, 0, xf[0], rs, nb);
  open_stage<8>();
  lptr base = lp(cur + ta);
  w0 = trd2(base, base + 1280); w1 = trd2(base + 64, base + 64 + 1280);
  if (NH == 0) w2 = trd2(base + 128, base + 128 + 1280);
#pragma unroll
  for (int s = 0; s < 10; ++s) {
#pragma unroll
    for (int t = 0; t < 4; ++t) {
      const int ks = 4 * s + t;
      SB0();
      if (s < 5) {
        if (t == 0) dma16(prv + w * 1024, kv0, rqkv, (s + 5) * 64 * 3840);
        if (t == 1) dma16(prv + (w + 8) * 1024, kv1, rqkv, (s + 5) * 64 * 3840);
        if (t == 2 && NH == 0) dma16(prv + (w + 16) * 1024, kv2, rqkv, (s + 5) * 64 * 3840);
      } else {
        if (t == 0) dma16(prv + w * 1024, guvo, rg_, (s - 5) * 64 * 3072 + (16 + ci) * 128);
        if (t == 1) dma16(prv + (w + 8) * 1024, guvo, ru_, (s - 5) * 64 * 3072 + (16 + ci) * 128);
      }
      if (t < 3 || s < 9) {
        if (t == 3) { open_stage<8>(); prv = cur; cur = nxt(cur); base = lp(cur + ta); }
        const lptr o = base + ((t + 1) & 3) * 5120;
        n0 = trd2(o, o + 1280); n1 = trd2(o + 64, o + 64 + 1280);
        if (NH == 0) n2 = trd2(o + 128, o + 128 + 1280);
      }
      SB0();
      a0 = MFMA32(asbf(xf[ks]), w0, a0);
      a1 = MFMA32(asbf(xf[ks]), w1, a1);
      if (NH == 0) a2 = MFMA32(asbf(xf[ks]), w2, a2);
      SB0();
      if (ks < 39) norm_frag_if(1, ks + 1, xf[ks + 1], rs, nb);
      w0 = n0; w1 = n1; if (NH == 0) w2 = n2;
    }
  }
  SB0();
  const int l2 = opq(tid) & 63;
  if (NH == 0) {
#pragma unroll
    for (int j = 0; j < 16; ++j) { a0[j] = phi(a0[j]); a1[j] = phi(a1[j]); float p = phi(a2[j]); a2[j] = (l2 & 16) ? a2[j] : p; }
  }
  const lptr e0 = lp(MISC + ((NH * 12 + (w & 3)) * 2) * 1024 + l2 * 16);
  lds_w128(e0, pack8(a0, 0)); lds_w128(e0 + 1024, pack8(a0, 1));
  lds_w128(e0 + 8192, pack8(a1, 0)); lds_w128(e0 + 8192 + 1024, pack8(a1, 1));
  if (NH == 0) { lds_w128(e0 + 16384, pack8(a2, 0)); lds_w128(e0 + 16384 + 1024, pack8(a2, 1)); }
}

template <int NH>
DEV void q_chunk(u32x4 (&xf)[40], uint32_t& cur, uint32_t& prv, int tid, int w, int ci, int rg, int ct, uint32_t fb_row,
                 rsrc_t rws, char* ws, u64 tag) {
  const int l = tid & 63, rb = w & 3;
  const uint32_t ta = (8 * (l >> 5) + ((l >> 2) & 3)) * 320 + ((l >> 4) & 1) * 32 + 8 * (l & 3) + NH * 128;
  const uint32_t ZP = MISC, ZS = MISC + 5120, X3 = MISC + 5632;
  const uint32_t l16 = l * 16;
  f32x16 a0 = {}, a1 = {};
  bf16x8 w0, w1, n0, n1;
  open_stage<8>();
  const int zt = min(tid, 319), zr = (zt * 3277) >> 16;
  u32x4 zp = aload128(rws, zr * KVREC + (zt - 20 * zr) * 16, WS_KVP + ci * 16 * KVREC + 6400);
  lptr base = lp(cur + ta);
  w0 = trd2(base, base + 1280);
  if (NH == 0) w1 = trd2(base + 64, base + 64 + 1280);
#pragma unroll
  for (int s = 0; s < 5; ++s) {
#pragma unroll
    for (int t = 0; t < 8; ++t) {
      const int ks = 8 * s + t;
      SB0();
      if (t < 2 || (t == 2 && NH == 0))
        dma16_sc1(prv + (w + 8 * t) * 1024, l16, rws, WS_W2 + (ct * 340 + s * 20 + w + 8 * t) * 1024);
      if (s == 2 && t == 0) {
        if (NH == 0 && tid == 0) flag_store(ws, WS_FB + rg * 128 + ci * 8, (tag & ~0xffull) | 2);
        fence_reg(zp); lds_w128(lp(ZP + zt * 16), zp);
      }
      if (NH == 1 && s == 3 && t == 0 && w < 6) {
        const int zi = min(tid - 256, 79);
        const lptr za = lp(ZP + zi * 4);
        float z = 0.f;
#pragma unroll
        for (int j = 0; j < 16; ++j) z += lds_rf(za + j * 320);
        lds_wf(za + 5120, z);
      }
      if (t < 7 || s < 4) {
        if (t == 7) {
          if (s == 1) open_stage<4>(); else open_stage<8>();
          prv = cur; cur = nxt(cur); base = lp(cur + ta);
        }
        const int tn = (t + 1) & 7;
        const lptr o = base + (tn & 3) * 5120 + (tn >> 2) * 160;
        n0 = trd2(o, o + 1280);
        if (NH == 0) n1 = trd2(o + 64, o + 64 + 1280);
      }
      SB0();
      a0 = MFMA32(w0, asbf(xf[ks]), a0);
      if (NH == 0) a1 = MFMA32(w1, asbf(xf[ks]), a1);
      w0 = n0; if (NH == 0) w1 = n1;
    }
  }
  SB0();
  prv = cur; cur = nxt(cur);
  STAMP(8);
  const int l2 = opq(tid) & 63, r = l2 & 31, hh = l2 >> 5;
  const uint32_t pv = (r >> 4) * (68 * 1024) + (hh * 16 + (r & 15)) * 16;
  const uint32_t qb = WS_A2 + ((rg * 8 + 2 * rb) * 68 + 48) * 1024 + ci * 5 * 512;
  const lptr zs = lp(ZS + 16 * hh), x3 = lp(X3 + (32 * rb + r) * 4);
  if (NH == 0) {
    u32x2 pl = aload64(rws, (l2 & 7) * 8, fb_row);
    float gs[8];
#pragma unroll
    for (int g = 0; g < 8; ++g) {
      const f32x4 z = lds_rf4(zs + 32 * g);
      float acc = 0.f;
#pragma unroll
      for (int j = 0; j < 4; ++j) {
        float v = phi(g < 4 ? a0[4 * g + j] : a1[4 * (g - 4) + j]);
        if (g < 4) a0[4 * g + j] = v; else a1[4 * (g - 4) + j] = v;
        acc = fmaf(v, z[j], acc);
      }
      gs[g] = acc;
    }
    float p0 = gs[0] + gs[1] + (hh ? 0.f : gs[2]);
    float p1 = gs[3] + gs[4] + (hh ? gs[2] : 0.f);
    float p2 = gs[5] + gs[6] + (hh ? 0.f : gs[7]);
    if (hh) lds_wf(x3, gs[7]);
    p0 += __shfl_xor(p0, 32); p1 += __shfl_xor(p1, 32); p2 += __shfl_xor(p2, 32);
    wait_vm<0>(); fence_reg(pl);
    if (w == 0) check_or_wait(pl, ws, fb_row + (l2 & 7) * 8, 2, tag);
    bar();
    const float p3 = gs[7] + lds_rf(x3 + 512);
    const float r0 = 1.f / (p0 + 1e-6f), r1 = 1.f / (p1 + 1e-6f), r2 = 1.f / (p2 + 1e-6f), r3 = 1.f / (p3 + 1e-6f);
    const float sc[8] = {r0, r0, hh ? r1 : r0, r1, r1, r2, r2, hh ? r3 : r2};
#pragma unroll
    for (int j = 0; j < 16; ++j) { a0[j] = a0[j] * sc[j >> 2]; a1[j] = a1[j] * sc[4 + (j >> 2)]; }
    st128_sc1(pack8(a0, 0), rws, pv, qb); st128_sc1(pack8(a0, 1), rws, pv, qb + 512);
    st128_sc1(pack8(a1, 0), rws, pv, qb + 1024); st128_sc1(pack8(a1, 1), rws, pv, qb + 1536);
  } else {
    float p3 = 0.f;
#pragma unroll
    for (int g = 0; g < 2; ++g) {
      const f32x4 z = lds_rf4(zs + 256 + 32 * g);
#pragma unroll
      for (int j = 0; j < 4; ++j) { float v = phi(a0[4 * g + j]); a0[4 * g + j] = v; p3 = fmaf(v, z[j], p3); }
    }
    p3 += __shfl_xor(p3, 32);
    if (hh) lds_wf(x3 + 512, p3);
    bar();
    p3 += lds_rf(x3);
    const float r3 = 1.f / (p3 + 1e-6f);
#pragma unroll
    for (int j = 0; j < 8; ++j) a0[j] = a0[j] * r3;
    st128_sc1(pack8(a0, 0), rws, pv, qb + 2048);
  }
}

__global__ __launch_bounds__(512, 2) void fused_kernel(const void* __restrict__ gx, const void* __restrict__ gwqkv,
                                                       const void* __restrict__ gwo, const void* __restrict__ gwg,
                                                       const void* __restrict__ gwu, const void* __restrict__ gwd,
                                                       void* __restrict__ gout, char* __restrict__ ws) {
  const int tid = threadIdx.x;
  const int w = __builtin_amdgcn_readfirstlane(tid >> 6);
  const int rb = w & 3, nh = w >> 2;
  const int b = blockIdx.x;
  const int xcd = b & 7, idx = b >> 3;
  const int rg = (xcd >> 2) * 8 + (idx >> 2);
  const bool iskv = (idx & 2) != 0;
  const int ci = 2 * (xcd & 3) + (idx & 1);
  const int tt = (xcd >> 1) * 8 + (idx >> 2), ct = (xcd & 1) * 4 + (idx & 3);

  const rsrc_t rx = mk_rsrc(gx, 2048 * 1280), rqkv = mk_rsrc(gwqkv, 640 * 3840);
  const rsrc_t rg_ = mk_rsrc(gwg, 640 * 3072), ru_ = mk_rsrc(gwu, 640 * 3072);
  const rsrc_t rws = mk_rsrc(ws, WS_USED);
  STAMP(0);

  uint32_t cur = 0, prv = 5 * SLOT;
  u64 tag;
  u32x4 xf[40];
  float rs, nb;
  uint32_t guvo;
  {
    const int l = tid & 63, hh = l >> 5, r = l & 31;
    u32x2 eld = aload64(rws, 0, WS_E);
    const int t0ks = b / 5, t0cg = b - 5 * t0ks;
    if (b < 240) dma16(MISC + w * 1024, (4 * w + (l >> 4)) * 1280 + (l & 15) * 16, mk_rsrc(gwd, 1536 * 1280), t0ks * 32 * 1280 + t0cg * 256);
    const uint32_t xvo = (8 * w + (l >> 3)) * 1280 + (((l & 7) ^ ((4 * w + (l >> 4)) & 7)) << 4);
    const uint32_t xso = rg * 128 * 1280;
#pragma unroll
    for (int s = 0; s < 5; ++s) {
      dma16(s * SLOT + w * 1024, xvo, rx, xso + s * 128);
      dma16(s * SLOT + (w + 8) * 1024, xvo, rx, xso + s * 128 + 64 * 1280);
    }
    guvo = (8 * w + (l >> 3)) * 3072 + (((l & 7) ^ (((l >> 4) & 1) << 2)) << 4);
    const uint32_t xra = (32 * rb + r) * 128 + ((hh ^ ((r >> 1) & 7)) << 4);
    float s0 = 0.f, s1 = 0.f, q0 = 0.f, q1 = 0.f;
    const uint32_t kv0 = iskv ? kvq_voff(w, l, ci, false) : guvo, kv1 = iskv ? kvq_voff(w + 8, l, ci, false) : guvo;
    const uint32_t kv2 = kvq_voff((w & 3) + 16, l, ci, false);
    const rsrc_t rf0 = iskv ? rqkv : rg_, rf1 = iskv ? rqkv : ru_;
    const uint32_t fstr = iskv ? 64 * 3840 : 64 * 3072, fso = iskv ? 0 : ci * 128, third = ((idx >> 1) & 1) & (nh ^ 1);
#pragma unroll
    for (int s = 0; s < 10; ++s) {
      open_stage<8>();
#pragma unroll
      for (int t = 0; t < 4; ++t) xf[4 * s + t] = lds_r128((lptr)(cur + (xra ^ (t << 5))));
      if (s < 5) {
        dma16(prv + w * 1024, xvo, rx, xso + (s + 5) * 128);
        dma16(prv + (w + 8) * 1024, xvo, rx, xso + (s + 5) * 128 + 64 * 1280);
      } else {
        dma16(prv + w * 1024, kv0, rf0, fso + (s - 5) * fstr);
        dma16(prv + (w + 8) * 1024, kv1, rf1, fso + (s - 5) * fstr);
        dma16_if(third, prv + (w + 16) * 1024, kv2, rqkv, (s - 5) * 64 * 3840);
      }
      if (s == 0) {
        fence_reg(eld);
        u64 e = ((u64)(uint32_t)__builtin_amdgcn_readfirstlane(eld[1]) << 32) | (uint32_t)__builtin_amdgcn_readfirstlane(eld[0]);
        tag = (e * 6364136223846793005ull + 1442695040888963407ull);
        if (b < 240) {
          const int g = l >> 4;
          const lptr a = lp(MISC + (16 * (g >> 1) + 4 * (g & 1) + ((l >> 2) & 3)) * 256 + w * 32 + 8 * (l & 3));
          bf16x8 f = trd2(a, a + 8 * 256);
          const int cb = 8 * t0cg + w, c5 = cb / 5, n5 = cb - 5 * c5;
          st128_sc1(__builtin_bit_cast(u32x4, f), rws, l * 16, WS_W2 + ((c5 * 68 + t0ks) * 5 + n5) * 1024);
        }
      }
      if (s == 6 && tid == 0) flag_store(ws, WS_FT + b * 8, (tag & ~0xffull) | 1);
#pragma unroll
      for (int t = 0; t < NDOT; ++t) {
        const uint32_t e0 = xf[4 * s + t][0], e1 = xf[4 * s + t][1], e2 = xf[4 * s + t][2], e3 = xf[4 * s + t][3];
        asm volatile("v_dot2c_f32_bf16 %0, %8, %4\n\tv_dot2c_f32_bf16 %2, %4, %4\n\tv_dot2c_f32_bf16 %1, %8, %5\n\tv_dot2c_f32_bf16 %3, %5, %5\n\t"
                     "v_dot2c_f32_bf16 %0, %8, %6\n\tv_dot2c_f32_bf16 %2, %6, %6\n\tv_dot2c_f32_bf16 %1, %8, %7\n\tv_dot2c_f32_bf16 %3, %7, %7"
                     : "+v"(s0), "+v"(s1), "+v"(q0), "+v"(q1) : "v"(e0), "v"(e1), "v"(e2), "v"(e3), "s"(0x3f803f80u));
      }
      SB0();
      prv = cur; cur = nxt(cur);
    }
    asm volatile("s_nop 3" : "+v"(s0), "+v"(s1), "+v"(q0), "+v"(q1));
    float sm = s0 + s1, sq = q0 + q1;
    sm += __shfl_xor(sm, 32); sq += __shfl_xor(sq, 32);
    const float mean = sm * (1.f / 640.f);
    const float var = fmaxf(sq * (1.f / 640.f) - mean * mean, 0.f);
    rs = 1.0f / sqrtf(var + 1e-5f);
    nb = -mean * rs;
    STAMP(1);

    if (iskv) {
      if (nh == 0) kv_chunk<0>(xf, rs, nb, cur, prv, opq(tid), w, ci, kv0, kv1, kv2, guvo, rqkv, rg_, ru_);
      if (opqs(nh) != 0) kv_chunk<1>(xf, rs, nb, cur, prv, opq(tid), w, ci, kv0, kv1, kv2, guvo, rqkv, rg_, ru_);
      STAMP(2);
      const int t2 = opq(tid), l = t2 & 63, hh = l >> 5, r = l & 31;
      u32x4 ft0, ft1;
      if (w == 7) { ft0 = aload128(rws, l * 32, WS_FT); ft1 = aload128(rws, l * 32, WS_FT + 16); }
      bar();
      const int kb = w < 5 ? (w >> 1) : (w - 5), vb = 2 + ((w + 1) >> 1);
      const lptr ka = lp(MISC + kb * 8192 + l * 16), va = lp(MISC + vb * 8192 + l * 16);
      f32x16 d = {};
      const uint32_t ck = cur;
      if (w < 5) {
#pragma unroll
        for (int j = 0; j < 8; ++j) d = MFMA32(asbf(lds_r128(ka + j * 1024)), asbf(lds_r128(va + j * 1024)), d);
        const int ve = 32 * vb + r - 80;
        const int hv = (ve * 205) >> 12;
        const int e = ve - 20 * hv;
        const int lo = 20 * hv - 32 * kb - 4 * hh;
        const uint32_t ca = ck + (hv * 400 + e - lo * 20) * 4;
#pragma unroll
        for (int j = 0; j < 16; ++j) {
          const int rowb = (j & 3) + 8 * (j >> 2);
          if (ve >= 0 && (unsigned)(rowb - lo) < 20u) lds_wf((lptr)(ca + rowb * 80), d[j]);
        }
      } else {
        const u32x4 ones = {0x3f803f80u, 0x3f803f80u, 0x3f803f80u, 0x3f803f80u};
#pragma unroll
        for (int j = 0; j < 8; ++j) d = MFMA32(asbf(ones), asbf(lds_r128(ka + j * 1024)), d);
        if (hh == 0 && 32 * kb + r < 80) lds_wf((lptr)(ck + 6400 + (32 * kb + r) * 4), d[0]);
      }
      if (w == 7) { wait_vm<0>(); fence_reg(ft0); fence_reg(ft1); check4(ft0, ft1, ws, WS_FT + l * 32, tag); }
      bar();
      if (t2 < 420) st128_sc1(lds_r128((lptr)(ck + t2 * 16)), rws, t2 * 16, WS_KVP + (ci * 16 + rg) * KVREC);
      prv = cur; cur = nxt(cur);
      STAMP(3);
    }
  }

  {
    const int ngu = iskv ? 1 : 2;
#pragma unroll 1
    for (int it = 0; it < ngu; ++it) {
      const int l = opq(tid) & 63;
      const uint32_t ga = (8 * (l >> 5) + ((l >> 2) & 3)) * 128 + 64 * (nh ^ ((l >> 3) & 1)) + 32 * ((l >> 4) & 1) + 8 * (l & 3);
      const uint32_t first = opqs((((idx >> 1) | it) & 1) ^ 1);
      const int c = iskv ? 16 + ci : it * 8 + ci;
      uint32_t nv0, nv1, nv2, nso, nstr; rsrc_t nr0, nr1;
      if (first) { nv0 = nv1 = nv2 = guvo; nr0 = rg_; nr1 = ru_; nso = (c + 8) * 128; nstr = 64 * 3072; }
      else if (!iskv) { nv0 = kvq_voff(w, l, ci, true); nv1 = kvq_voff(w + 8, l, ci, true); nv2 = kvq_voff((w & 3) + 16, l, ci, true);
                        nr0 = nr1 = rqkv; nso = 0; nstr = 128 * 3840; }
      else { nv0 = l * 16; nv1 = l * 16 + 8192; nv2 = l * 16 + 16384; nr0 = nr1 = rws; nso = WS_W2 + (ct * 340 + w) * 1024; nstr = 20480; }
      nso = __builtin_amdgcn_readfirstlane(nso); nstr = __builtin_amdgcn_readfirstlane(nstr);
      const uint32_t third = (first | nh) ^ 1;
      const uint32_t gso = opqs(c * 128);
      f32x16 ag = {}, au = {};
      bf16x8 w0, w1, n0, n1;
      norm_frag_if(first, 0, xf[0], rs, nb);
      open_stage<8>();
      lptr base = lp(cur + ga);
      w0 = trd2(base, base + 512); w1 = trd2(base + 8192, base + 8192 + 512);
#pragma unroll
      for (int s = 0; s < 10; ++s) {
#pragma unroll
        for (int t = 0; t < 4; ++t) {
          const int ks = 4 * s + t;
          SB0();
          if (s < 5) {
            if (t == 0) dma16(prv + w * 1024, guvo, rg_, (s + 5) * 64 * 3072 + gso);
            if (t == 1) dma16(prv + (w + 8) * 1024, guvo, ru_, (s + 5) * 64 * 3072 + gso);
          } else {
            if (t == 0) dma16_sc1(prv + w * 1024, nv0, nr0, nso);
            if (t == 1) dma16_sc1(prv + (w + 8) * 1024, nv1, nr1, nso);
            if (t == 2) { dma16_if(third, prv + (w + 16) * 1024, nv2, nr1, nso); nso += nstr; }
          }
          if (s == 4 && t == 0 && iskv && tid == 0) flag_store(ws, WS_FA + (ci * 16 + rg) * 8, (tag & ~0xffull) | 1);
          if (t < 3 || s < 9) {
            if (t == 3) {
 open_stage<8>();
 prv = cur; cur = nxt(cur); base = lp(cur + ga); }
            const lptr o = base + ((t + 1) & 3) * 2048;
            n0 = trd2(o, o + 512); n1 = trd2(o + 8192, o + 8192 + 512);
          }
          SB0();
          ag = MFMA32(w0, asbf(xf[ks]), ag);
          au = MFMA32(w1, asbf(xf[ks]), au);
          SB0();
          if (ks < 39) norm_frag_if(first, ks + 1, xf[ks + 1], rs, nb);
          w0 = n0; w1 = n1;
        }
      }
      SB0();
      prv = cur; cur = nxt(cur);
      STAMP(4 + 2 * it);
      const int l2 = opq(tid) & 63, r = l2 & 31, hh = l2 >> 5;
      const uint32_t fvo = first ? l2 * 32 : (l2 & 3) * 32, fso = first ? WS_FT : WS_FA + ci * 128;
      u32x4 ft0, ft1;
      if (w == 0) { ft0 = aload128(rws, fvo, fso); ft1 = aload128(rws, fvo, fso + 16); }
#pragma unroll
      for (int j = 0; j < 16; ++j) ag[j] = ag[j] * au[j];
      const uint32_t hv = (r >> 4) * (68 * 1024) + (hh * 16 + (r & 15)) * 16;
      const uint32_t hs = WS_A2 + ((rg * 8 + 2 * rb) * 68 + 2 * c + nh) * 1024;
      st128_sc1(pack8(ag, 0), rws, hv, hs);
      st128_sc1(pack8(ag, 1), rws, hv + 512, hs);
      if (w == 0) {
        wait_vm<2>(); fence_reg(ft0); fence_reg(ft1);
        check4(ft0, ft1, ws, fso + fvo, tag);
        if (b == 0 && first && l2 == 0) flag_store(ws, WS_E, tag);
      }
      STAMP(5 + 2 * it);
    }
  }

  uint32_t pub_off, pub_lvl;
  const uint32_t fb_row = WS_FB + (tt >> 1) * 128;
  if (!iskv) {
    if (nh == 0) q_chunk<0>(xf, cur, prv, opq(tid), w, ci, rg, ct, fb_row, rws, ws, tag);
    else q_chunk<1>(xf, cur, prv, opq(tid), w, ci, rg, ct, fb_row, rws, ws, tag);
    pub_off = WS_FB + rg * 128 + ci * 8; pub_lvl = 3;
  } else {
    const int hl = rg & 3, qd = rg >> 2, head = 4 * ci + hl;
    const int tid = opq(threadIdx.x), l = tid & 63, q4 = (l >> 2) & 3, p4 = l & 3;
    const uint32_t WO = MISC, RED = MISC + 6400;
    bar();
    u32x2 pl; u32x4 wo4, k0, k1, k2, k3;
    if (w == 0) pl = aload64(rws, (l & 7) * 8, fb_row);
    if (tid < 400) {
      const int row = (tid * 3277) >> 16, rq = (tid * 656) >> 16;
      wo4 = aload128p(mk_rsrc(gwo, 640 * 1280), row * 1280 + (tid - 20 * row) * 16, head * 20 * 1280 + qd * 320);
      const uint32_t vo = rq * 4 * KVREC + (tid - 100 * rq) * 16, so = WS_KVP + ci * 16 * KVREC + hl * 1600;
      k0 = aload128(rws, vo, so); k1 = aload128(rws, vo, so + KVREC);
      k2 = aload128(rws, vo, so + 2 * KVREC); k3 = aload128(rws, vo, so + 3 * KVREC);
    }
    wait_vm<0>();
    if (tid < 400) {
      fence_reg(wo4); fence_reg(k0); fence_reg(k1); fence_reg(k2); fence_reg(k3);
      lds_w128((lptr)(WO + tid * 16), wo4);
      f32x4 sum = (__builtin_bit_cast(f32x4, k0) + __builtin_bit_cast(f32x4, k1)) + (__builtin_bit_cast(f32x4, k2) + __builtin_bit_cast(f32x4, k3));
      lds_w128((lptr)(RED + tid * 16), __builtin_bit_cast(u32x4, sum));
    }
    if (w == 0) { fence_reg(pl); check_or_wait(pl, ws, fb_row + (l & 7) * 8, 2, tag); }
    bar();
    if (tid == 0) flag_store(ws, WS_FB + rg * 128 + (8 + ci) * 8, (tag & ~0xffull) | 1);
    STAMP(8);
    {
      const int i16 = l & 15, g = l >> 4;
      u32x4 af[2];
#pragma unroll
      for (int db = 0; db < 2; ++db) {
        const int d = min(16 * db + i16, 19);
        f32x4 lo = {}, hi = {};
        const lptr al = lp(RED + d * 80 + min(g, 2) * 32), ah = lp(RED + d * 80 + min(g, 2) * 32 + (g < 2 ? 16 : 0));
#pragma unroll
        for (int j = 0; j < 4; ++j) {
          lo += lds_rf4(al + j * 1600);
          hi += lds_rf4(ah + j * 1600);
        }
        af[db][0] = g < 3 ? pk2(lo[0], lo[1]) : 0u; af[db][1] = g < 3 ? pk2(lo[2], lo[3]) : 0u;
        af[db][2] = g < 2 ? pk2(hi[0], hi[1]) : 0u; af[db][3] = g < 2 ? pk2(hi[2], hi[3]) : 0u;
      }
      const uint32_t b0 = WO + min(8 * g + q4, 16 + q4) * 320 + 8 * p4, b1 = WO + min(8 * g + 4 + q4, 16 + q4) * 320 + 8 * p4;
      for (int n = w; n < 10; n += 8) {
        const bf16x8 bf = trd2((lptr)(b0 + n * 32), (lptr)(b1 + n * 32));
        const f32x4 z4 = {};
        const f32x4 m0 = MFMA16(asbf(af[0]), bf, z4), m1 = MFMA16(asbf(af[1]), bf, z4);
        const int cb = 10 * qd + n, c5 = cb / 5, n5 = cb - 5 * c5;
        const uint32_t fb = WS_W2 + (c5 * 340 + 240 + n5) * 1024 + i16 * 16;
        const int G0 = 5 * head + g, G1 = 5 * head + 4;
        const uint32_t o0 = fb + (G0 >> 3) * 5120 + (((G0 >> 2) & 1) * 2 + (G0 & 1)) * 256 + ((G0 >> 1) & 1) * 8;
        const uint32_t o1 = fb + (G1 >> 3) * 5120 + (((G1 >> 2) & 1) * 2 + (G1 & 1)) * 256 + ((G1 >> 1) & 1) * 8;
        u32x2 v0 = {pk2(m0[0], m0[1]), pk2(m0[2], m0[3])}, v1 = {pk2(m1[0], m1[1]), pk2(m1[2], m1[3])};
        __builtin_amdgcn_raw_buffer_store_b64(v0, rws, o0, 0, 16);
        if (g == 0) __builtin_amdgcn_raw_buffer_store_b64(v1, rws, o1, 0, 16);
      }
    }
    pub_off = WS_FM + (qd * 32 + head) * 8; pub_lvl = 1;
  }
  STAMP(9);

  {
    const int kh = nh;
    const int tid = opq(threadIdx.x), l = tid & 63;
    u32x4 A[34];
    const int pr0 = (tid * 6554) >> 16;
    const int pr1 = ((tid + 512) * 6554) >> 16;
    const uint32_t xo0 = pr0 * 1280 + (tid - 10 * pr0) * 16, xo1 = pr1 * 1280 + (tid + 512 - 10 * pr1) * 16;
    const uint32_t xs = tt * 64 * 1280 + ct * 160;
    u32x4 xr0 = aload128p(rx, xo0, xs), xr1;
    if (tid < 128) xr1 = aload128p(rx, xo1, xs);
    const uint32_t as = WS_A2 + ((tt * 4 + rb) * 68 + 2 * kh) * 1024;
    const uint32_t l16 = l * 16;
#pragma unroll
    for (int p = 0; p < 5; ++p) { A[2 * p] = aload128(rws, l16, as + p * 4096); A[2 * p + 1] = aload128(rws, l16, as + p * 4096 + 1024); }
    u32x2 pl;
    const uint32_t po2 = fb_row + 64 + (l & 7) * 8;
    const uint32_t po3 = l < 32 ? WS_FM + (ct >> 1) * 256 + l * 8 : fb_row + (l & 7) * 8;
    if (w == 0) pl = aload64(rws, po2, 0);
    f32x4 acc[5] = {};
#pragma unroll
    for (int p = 0; p < 17; ++p) {
      const int lo = p < 5 ? 0 : p - 4, hi = p - 1 < 11 ? p - 1 : 11;
      const int nop = hi >= lo ? hi - lo + 1 : 0, ex = p < 5 ? 2 * (4 - p) : 0;
      if (p == 3 || p == 7) {
        if (w == 0) {
          wait_vm<15>(); fence_reg(pl);
          if (p == 3) check_or_wait(pl, ws, po2, 1, tag); else check_or_wait(pl, ws, po3, l < 32 ? 1 : 3, tag);
        }
      }
      switch (nop * 16 + ex) {
#define OPEN_CASE(NOP, EX) case NOP * 16 + EX: open_stage2<NOP * 5 + EX, NOP * 4 + EX>(nh); break;
        OPEN_CASE(0, 8) OPEN_CASE(1, 6) OPEN_CASE(2, 4) OPEN_CASE(3, 2) OPEN_CASE(4, 0)
        OPEN_CASE(3, 0) OPEN_CASE(2, 0) OPEN_CASE(1, 0) OPEN_CASE(0, 0)
        default: open_stage<0>();
      }
      fence_reg(A[2 * p]); fence_reg(A[2 * p + 1]);
      if (p == 0 && tid == 0) flag_store(ws, pub_off, (tag & ~0xffull) | pub_lvl);
      const lptr rbs = lp(cur + kh * 10240 + l16);
      u32x4 B[10];
#pragma unroll
      for (int j = 0; j < 10; ++j) B[j] = lds_r128(rbs + j * 1024);
      SB0();
      if (p + 5 < 17) {
        const uint32_t so = WS_W2 + (ct * 340 + (p + 5) * 20 + w) * 1024;
        dma16_sc1(prv + w * 1024, l16, rws, so);
        dma16_sc1(prv + (w + 8) * 1024, l16, rws, so + 8192);
        dma16_if(nh ^ 1, prv + (w + 16) * 1024, l16, rws, so + 16384);
        A[2 * p + 10] = aload128(rws, l16, as + (p + 5) * 4096);
        A[2 * p + 11] = aload128(rws, l16, as + (p + 5) * 4096 + 1024);
      }
      if (p == 3 && w == 0) pl = aload64(rws, po3, 0);
      SB0();
#pragma unroll
      for (int j = 0; j < 10; ++j) acc[j % 5] = MFMA16(asbf(B[j]), asbf(A[2 * p + j / 5]), acc[j % 5]);
      SB0();
      prv = cur; cur = nxt(cur);
    }
    STAMP(10);
    bar();
    const lptr ow = lp((kh * 64 + rb * 16 + (l & 15)) * 336 + (l >> 4) * 16);
#pragma unroll
    for (int j = 0; j < 5; ++j) lds_w128(ow + j * 64, __builtin_bit_cast(u32x4, acc[j]));
    bar();
    const rsrc_t ro = mk_rsrc(gout, 2048 * 1280);
#pragma unroll
    for (int h = 0; h < 2; ++h) {
      if (h == 0 || tid < 128) {
        u32x4 xr = h ? xr1 : xr0;
        fence_reg(xr);
        const int pr = h ? pr1 : pr0, pc = tid + 512 * h - 10 * pr;
        const lptr a = lp(pr * 336 + pc * 32);
        f32x4 u0 = lds_rf4(a) + lds_rf4(a + 64 * 336);
        f32x4 u1 = lds_rf4(a + 16) + lds_rf4(a + 16 + 64 * 336);
        u32x4 o;
        o[0] = pk2(u0[0] + blo(xr[0]), u0[1] + bhi(xr[0])); o[1] = pk2(u0[2] + blo(xr[1]), u0[3] + bhi(xr[1]));
        o[2] = pk2(u1[0] + blo(xr[2]), u1[1] + bhi(xr[2])); o[3] = pk2(u1[2] + blo(xr[3]), u1[3] + bhi(xr[3]));
        __builtin_amdgcn_raw_buffer_store_b128(o, ro, h ? xo1 : xo0, xs, 0);
      }
    }
    STAMP(11);
  }
}

__global__ void pre_kernel(char* ws) {
  if (threadIdx.x == 0) ((u64*)(ws + (13u << 20)))[0] = __builtin_amdgcn_s_memrealtime();
}
__global__ void dbg_kernel(char* ws) {
  u64 now = __builtin_amdgcn_s_memrealtime();
  const u64* s = (const u64*)(ws + (14u << 20));
  const int l = threadIdx.x;
  u64 t0 = ~0ull, t1 = 0;
  for (int j = 0; j < 4; ++j) { u64 a = s[(l * 4 + j) * 32]; t0 = a < t0 ? a : t0; a = s[(l * 4 + j) * 32 + 11]; t1 = a > t1 ? a : t1; }
  for (int o = 1; o < 64; o <<= 1) { u64 a = __shfl_xor(t0, o); t0 = a < t0 ? a : t0; a = __shfl_xor(t1, o); t1 = a > t1 ? a : t1; }
  u64 tp = ((u64*)(ws + (13u << 20)))[0];
  u64 n = SI == 0 ? 0 : SI == 1 ? (now - t1) * 4 : (t0 - tp) * 4;
  if (n > 20000) n = 20000;
  u64 st = __builtin_amdgcn_s_memrealtime();
  while (__builtin_amdgcn_s_memrealtime() - st < n) {}
}
extern "C" void kernel_launch(void* const* d_in, const int* in_sizes, int n_in, void* d_out, int out_size, void* d_ws,
                              size_t ws_size, hipStream_t stream) {
  (void)in_sizes; (void)n_in; (void)out_size; (void)ws_size;
  (void)hipFuncSetAttribute((const void*)fused_kernel, hipFuncAttributeMaxDynamicSharedMemorySize, LDS_BYTES);
  pre_kernel<<<1,64,0,stream>>>((char*)d_ws);
  fused_kernel<<<256, 512, LDS_BYTES, stream>>>(d_in[0], d_in[1], d_in[2], d_in[4], d_in[5], d_in[6], d_out, (char*)d_ws);
  dbg_kernel<<<1,64,0,stream>>>((char*)d_ws);
}
```

```cpp
#include <hip/hip_runtime.h>
#include <stdint.h>

typedef __attribute__((ext_vector_type(8))) __bf16 bf16x8;
typedef __attribute__((ext_vector_type(4))) __bf16 bf16x4;
typedef __attribute__((ext_vector_type(2))) __bf16 bf16x2;
typedef __attribute__((ext_vector_type(16))) float f32x16;
typedef __attribute__((ext_vector_type(4))) float f32x4;
typedef __attribute__((ext_vector_type(2))) float f32x2;
typedef __attribute__((ext_vector_type(4))) unsigned u32x4;
typedef __attribute__((ext_vector_type(2))) unsigned u32x2;
typedef __amdgpu_buffer_rsrc_t rsrc_t;
typedef unsigned long long u64;
#define AS3 __attribute__((address_space(3)))
#define AS1 __attribute__((address_space(1)))
#define DEV __device__ __forceinline__
#define SB0() __builtin_amdgcn_sched_barrier(0)

constexpr uint32_t SLOT = 20480, RING = 122880, MISC = RING, LDS_BYTES = 163840;
constexpr uint32_t WS_E = 0, WS_FT = 4096, WS_FA = 8192, WS_FB = 12288, WS_FM = 16384;
constexpr uint32_t WS_KVP = 32768, KVREC = 6912;
constexpr uint32_t WS_W2 = 1u << 20, WS_A2 = 4u << 20;
constexpr uint32_t WS_USED = WS_A2 + 128u * 68u * 1024u;
constexpr uint32_t SPIN_CAP = 1u << 16;

DEV rsrc_t mk_rsrc(const void* p, uint32_t bytes) {
  return __builtin_amdgcn_make_buffer_rsrc((void*)p, 0, bytes, 0x00020000);
}
DEV void dma16(uint32_t m0v, uint32_t voff, rsrc_t srd, uint32_t soff) {
  asm volatile("s_mov_b32 m0, %0\n\ts_nop 0\n\tbuffer_load_dwordx4 %1, %2, %3 offen lds"
               :: "s"(m0v), "v"(voff), "s"(srd), "s"(soff) : "memory");
}
DEV void dma16_sc1(uint32_t m0v, uint32_t voff, rsrc_t srd, uint32_t soff) {
  asm volatile("s_mov_b32 m0, %0\n\ts_nop 0\n\tbuffer_load_dwordx4 %1, %2, %3 offen sc1 lds"
               :: "s"(m0v), "v"(voff), "s"(srd), "s"(soff) : "memory");
}
DEV void dma16_if(uint32_t cond, uint32_t m0v, uint32_t voff, rsrc_t srd, uint32_t soff) {
  asm volatile("s_cmp_eq_u32 %4, 0\n\ts_cbranch_scc1 1f\n\ts_mov_b32 m0, %0\n\ts_nop 0\n\t"
               "buffer_load_dwordx4 %1, %2, %3 offen sc1 lds\n1:"
               :: "s"(m0v), "v"(voff), "s"(srd), "s"(soff), "s"(cond) : "memory", "scc");
}
template <int N> DEV void wait_vm() { asm volatile("s_waitcnt vmcnt(%0)" :: "n"(N) : "memory"); }
DEV void bar() { asm volatile("s_waitcnt lgkmcnt(0)\n\ts_barrier" ::: "memory"); }
template <int N> DEV void open_stage() {
  asm volatile("s_waitcnt vmcnt(%0) lgkmcnt(0)\n\ts_barrier" :: "n"(N) : "memory");
}
template <int N0, int N1> DEV void open_stage2(uint32_t sel) {
  asm volatile("s_cmp_eq_u32 %2, 0\n\ts_cbranch_scc1 1f\n\ts_waitcnt vmcnt(%1)\n1:\n\ts_waitcnt vmcnt(%0) lgkmcnt(0)\n\ts_barrier"
               :: "n"(N0), "n"(N1), "s"(sel) : "memory", "scc");
}
constexpr uint32_t PF = MISC + 40960 - 32;
struct Sync { uint32_t g; uint32_t pfr; u64 mask; };
template <int N0, int N1> DEV void arrive2(const Sync& y, uint32_t sel) {
  uint32_t t;
  asm volatile("s_cmp_eq_u32 %6, 0\n\ts_cbranch_scc1 1f\n\ts_waitcnt vmcnt(%5)\n1:\n\ts_waitcnt vmcnt(%4)\n\tv_mov_b32 %0, %1\n\t"
               "s_mov_b64 exec, %2\n\tds_write_b32 %3, %0\n\ts_mov_b64 exec, -1"
               : "=&v"(t) : "s"(y.g), "s"(y.mask), "v"(y.pfr), "n"(N0), "n"(N1), "s"(sel) : "memory", "scc");
}
template <int N> DEV void arrive(const Sync& y) {
  uint32_t t;
  asm volatile("s_waitcnt vmcnt(%4)\n\tv_mov_b32 %0, %1\n\ts_mov_b64 exec, %2\n\tds_write_b32 %3, %0\n\ts_mov_b64 exec, -1"
               : "=&v"(t) : "s"(y.g), "s"(y.mask), "v"(y.pfr), "n"(N) : "memory");
}
DEV uint32_t flagread(const Sync& y) {
  uint32_t f; asm volatile("ds_read_b32 %0, %1" : "=v"(f) : "v"(y.pfr) : "memory"); return f;
}
DEV void check(Sync& y, uint32_t f) {
  asm volatile("s_waitcnt lgkmcnt(0)\n\tv_cmp_gt_u32 vcc, %1, %0\n\ts_cbranch_vccz 2f\n1:\n\tds_read_b32 %0, %2\n\t"
               "s_waitcnt lgkmcnt(0)\n\tv_cmp_gt_u32 vcc, %1, %0\n\ts_cbranch_vccnz 1b\n2:"
               : "+v"(f) : "s"(y.g), "v"(y.pfr) : "memory", "vcc");
  y.g += 1;
}
DEV void publish_if0(uint32_t cond, rsrc_t r, uint32_t soff, u64 val) {
  u64 t;
  asm volatile("s_cmp_lg_u32 %4, 0\n\ts_cbranch_scc1 1f\n\tv_mov_b64 %0, %1\n\ts_mov_b64 exec, 1\n\t"
               "buffer_store_dwordx2 %0, off, %2, %3 sc1\n\ts_mov_b64 exec, -1\n1:"
               : "=&v"(t) : "s"(val), "s"(r), "s"(soff), "s"(cond) : "memory", "scc");
}
typedef AS3 char* lptr;
DEV lptr lp(uint32_t a) { asm volatile("" : "+v"(a)); return (lptr)a; }
DEV bf16x4 trd(lptr a) { return __builtin_amdgcn_ds_read_tr16_b64_v4bf16((AS3 bf16x4*)a); }
DEV bf16x8 trd2(lptr a0, lptr a1) {
  bf16x4 lo = trd(a0), hi = trd(a1);
  return __builtin_shufflevector(lo, hi, 0, 1, 2, 3, 4, 5, 6, 7);
}
DEV u32x4 lds_r128(lptr a) { return *(AS3 u32x4*)a; }
DEV f32x4 lds_rf4(lptr a) { return *(AS3 f32x4*)a; }
DEV void lds_w128(lptr a, u32x4 v) { *(AS3 u32x4*)a = v; }
DEV float lds_rf(lptr a) { return *(AS3 float*)a; }
DEV void lds_wf(lptr a, float v) { *(AS3 float*)a = v; }
DEV int opq(int v) { asm volatile("" : "+v"(v)); return v; }
DEV uint32_t opqs(uint32_t v) { asm volatile("" : "+s"(v)); return v; }
DEV uint32_t pk2(float a, float b) {
  f32x2 v = {a, b};
  return __builtin_bit_cast(uint32_t, __builtin_convertvector(v, bf16x2));
}
DEV float blo(uint32_t u) { return __builtin_bit_cast(float, u << 16); }
DEV float bhi(uint32_t u) { return __builtin_bit_cast(float, u & 0xffff0000u); }
DEV float phi(float t) {
  float e = __builtin_amdgcn_exp2f(t * 1.4426950408889634f);
  e = __builtin_amdgcn_fmed3f(e, 0.f, 1.f);
  return fmaxf(t, 0.f) + e + 1e-6f;
}
#define MFMA32(a, b, c) __builtin_amdgcn_mfma_f32_32x32x16_bf16(a, b, c, 0, 0, 0)
#define MFMA16(a, b, c) __builtin_amdgcn_mfma_f32_16x16x32_bf16(a, b, c, 0, 0, 0)
DEV bf16x8 asbf(u32x4 v) { return __builtin_bit_cast(bf16x8, v); }
DEV u32x4 pack8(const f32x16& a, int s) {
  u32x4 r;
  r[0] = pk2(a[8 * s + 0], a[8 * s + 1]); r[1] = pk2(a[8 * s + 2], a[8 * s + 3]);
  r[2] = pk2(a[8 * s + 4], a[8 * s + 5]); r[3] = pk2(a[8 * s + 6], a[8 * s + 7]);
  return r;
}
DEV void st128_sc1(u32x4 v, rsrc_t r, uint32_t voff, uint32_t soff) {
  __builtin_amdgcn_raw_buffer_store_b128(v, r, voff, soff, 16);
}
DEV void flag_store(char* ws, uint32_t off, u64 v) {
  __hip_atomic_store((AS1 u64*)(ws + off), v, __ATOMIC_RELAXED, __HIP_MEMORY_SCOPE_AGENT);
}
DEV u64 flag_load(char* ws, uint32_t off) {
  return __hip_atomic_load((AS1 u64*)(ws + off), __ATOMIC_RELAXED, __HIP_MEMORY_SCOPE_AGENT);
}
DEV bool flag_ok(u64 v, u64 tag, uint32_t lvl) { return ((v ^ tag) >> 8) == 0 && (uint32_t)(v & 0xff) >= lvl; }
DEV u32x2 aload64(rsrc_t r, uint32_t voff, uint32_t soff) {
  u32x2 v; asm volatile("buffer_load_dwordx2 %0, %1, %2, %3 offen sc1" : "=v"(v) : "v"(voff), "s"(r), "s"(soff) : "memory"); return v;
}
DEV u32x4 aload128(rsrc_t r, uint32_t voff, uint32_t soff) {
  u32x4 v; asm volatile("buffer_load_dwordx4 %0, %1, %2, %3 offen sc1" : "=v"(v) : "v"(voff), "s"(r), "s"(soff) : "memory"); return v;
}
DEV u32x4 aload128p(rsrc_t r, uint32_t voff, uint32_t soff) {
  u32x4 v; asm volatile("buffer_load_dwordx4 %0, %1, %2, %3 offen" : "=v"(v) : "v"(voff), "s"(r), "s"(soff) : "memory"); return v;
}
template <typename T> DEV void fence_reg(T& v) { asm volatile("" : "+v"(v) :: "memory"); }
DEV u64 u64of(u32x2 v) { return ((u64)v[1] << 32) | v[0]; }
DEV void poll_wait(char* ws, uint32_t off, uint32_t lvl, u64 tag) {
  for (uint32_t it = 0; it < SPIN_CAP; ++it) {
    u64 v = flag_load(ws, off);
    if (__all(flag_ok(v, tag, lvl))) break;
    __builtin_amdgcn_s_sleep(1);
  }
}
DEV void check_or_wait(u32x2 got, char* ws, uint32_t off, uint32_t lvl, u64 tag) {
  if (!__all(flag_ok(u64of(got), tag, lvl))) poll_wait(ws, off, lvl, tag);
}
#define STAMP(i) do {} while (0)
#define PINX(I, A, D) case I: if (RW) asm volatile("" : "+{v[" #A ":" #D "]}"(f)); else asm volatile("" :: "{v[" #A ":" #D "]}"(f)); break;
template <bool RW> DEV void pin(int i, u32x4& f) {
  switch (i) {
    PINX(0, 96, 99) PINX(1, 100, 103) PINX(2, 104, 107) PINX(3, 108, 111) PINX(4, 112, 115)
    PINX(5, 116, 119) PINX(6, 120, 123) PINX(7, 124, 127) PINX(8, 128, 131) PINX(9, 132, 135)
    PINX(10, 136, 139) PINX(11, 140, 143) PINX(12, 144, 147) PINX(13, 148, 151) PINX(14, 152, 155)
    PINX(15, 156, 159) PINX(16, 160, 163) PINX(17, 164, 167) PINX(18, 168, 171) PINX(19, 172, 175)
    PINX(20, 176, 179) PINX(21, 180, 183) PINX(22, 184, 187) PINX(23, 188, 191) PINX(24, 192, 195)
    PINX(25, 196, 199) PINX(26, 200, 203) PINX(27, 204, 207) PINX(28, 208, 211) PINX(29, 212, 215)
    PINX(30, 216, 219) PINX(31, 220, 223) PINX(32, 224, 227) PINX(33, 228, 231) PINX(34, 232, 235)
    PINX(35, 236, 239) PINX(36, 240, 243) PINX(37, 244, 247) PINX(38, 248, 251) PINX(39, 252, 255)
  }
}
DEV bf16x8 split3(float v, bool side) {
  const uint32_t h = __builtin_bit_cast(uint32_t, v);
  const float r1 = v - bhi(h);
  const uint32_t m = __builtin_bit_cast(uint32_t, r1);
  const uint32_t l = __builtin_bit_cast(uint32_t, r1 - bhi(m));
  u32x4 o;
#define PK(LO, HI) __builtin_amdgcn_perm(HI, LO, 0x07060302)
  if (side) { o[0] = PK(h, h); o[1] = PK(m, h); o[2] = PK(l, m); o[3] = PK(m, l); }
  else { o[0] = PK(h, m); o[1] = PK(h, l); o[2] = PK(h, m); o[3] = PK(l, m); }
  return asbf(o);
}
DEV float sum4(f32x4 v) { return (v[0] + v[1]) + (v[2] + v[3]); }
DEV uint32_t nxt(uint32_t s) { return s + SLOT == RING ? 0u : s + SLOT; }
constexpr uint32_t SJA = 4 * SLOT + 16384, RSV = 16384;
constexpr uint32_t WS_CSG = 917504, WS_STG = 997376, OOB = 0x7ffffff0u;
template <int CTRL> DEV float dpp_add(float v) {
  return v + __builtin_bit_cast(float, __builtin_amdgcn_update_dpp(0, __builtin_bit_cast(int, v), CTRL, 0xf, 0xf, true));
}
DEV float wave_sum63(float v) {
  v = dpp_add<0x111>(v); v = dpp_add<0x112>(v); v = dpp_add<0x114>(v); v = dpp_add<0x118>(v);
  v += __builtin_bit_cast(float, __builtin_amdgcn_update_dpp(0, __builtin_bit_cast(int, v), 0x142, 0xa, 0xf, true));
  v += __builtin_bit_cast(float, __builtin_amdgcn_update_dpp(0, __builtin_bit_cast(int, v), 0x143, 0xc, 0xf, true));
  return v;
}
DEV void gran_fix(u32x2& g, char* ws, uint32_t off, uint32_t t32) {
  for (uint32_t it = 0; it < SPIN_CAP && !__all(g[1] == t32); ++it) {
    const u64 v = flag_load(ws, off);
    g[0] = (uint32_t)v; g[1] = (uint32_t)(v >> 32);
  }
}

DEV uint32_t kvq_voff(int piece, int lane, int i, bool isq) {
  uint32_t p = piece * 1024 + lane * 16;
  uint32_t row = (p * 52429u) >> 24;
  uint32_t col = p - row * 320;
  bool hi = col >= 160;
  uint32_t c = hi ? col - 160 : col;
  return isq ? (row + (hi ? 64u : 0u)) * 3840 + 160 * i + c
             : row * 3840 + (hi ? 2560u : 1280u) + 160 * i + c;
}
DEV void check4(u32x4 ft0, u32x4 ft1, char* ws, uint32_t off, u64 tag) {
  u32x2 f0 = {ft0[0], ft0[1]}, f1 = {ft0[2], ft0[3]}, f2 = {ft1[0], ft1[1]}, f3 = {ft1[2], ft1[3]};
  bool ok = flag_ok(u64of(f0), tag, 1) && flag_ok(u64of(f1), tag, 1) && flag_ok(u64of(f2), tag, 1) && flag_ok(u64of(f3), tag, 1);
  if (!__all(ok)) {
#pragma unroll 1
    for (int j = 0; j < 4; ++j) poll_wait(ws, off + 8 * j, 1, tag);
  }
}
DEV void h_store(f32x16& ag, f32x16& au, float cg, float cu, float mean, float rs, int tid, rsrc_t rws, uint32_t hs) {
  const bf16x8 mf = split3(-mean, false);
  ag = MFMA32(split3(cg, true), mf, ag);
  au = MFMA32(split3(cu, true), mf, au);
  const float r2 = rs * rs;
#pragma unroll
  for (int j = 0; j < 16; ++j) ag[j] = (ag[j] * r2) * au[j];
  const int l2 = tid & 63, r = l2 & 31, hh = l2 >> 5;
  const uint32_t hv = (r >> 4) * (68 * 1024) + (hh * 16 + (r & 15)) * 16;
  st128_sc1(pack8(ag, 0), rws, hv, hs);
  st128_sc1(pack8(ag, 1), rws, hv + 512, hs);
}

DEV void acheck(u32x2 f, rsrc_t r, uint32_t voff, uint32_t soff, u64 lo) {
  u64 m; uint32_t n;
  asm volatile("s_mov_b32 %2, 0x10000\n\ts_branch 2f\n1:\n\tbuffer_load_dwordx2 %0, %3, %4, %5 offen sc1\n\ts_waitcnt vmcnt(0)\n2:\n\t"
               "v_cmp_gt_u64 vcc, %6, %0\n\tv_cmp_lt_u64 %1, %7, %0\n\ts_sub_u32 %2, %2, 1\n\ts_cselect_b64 %1, 0, %1\n\ts_cselect_b64 vcc, 0, vcc\n\t"
               "s_or_b64 %1, %1, vcc\n\ts_cbranch_scc1 1b"
               : "+v"(f), "=&s"(m), "=&s"(n) : "v"(voff), "s"(r), "s"(soff), "s"(lo), "s"(lo | 0xff) : "memory", "vcc", "scc");
}
template <int MODE>
DEV void first_chunk(u32x4 (&xf)[40], float& mean, float& rs, u64& tag, int tid, int w, int b, int rg, int ci, rsrc_t rx,
                     rsrc_t rq, rsrc_t rg_, rsrc_t ru_, const void* gwd, rsrc_t rws, char* ws) {
  constexpr int D = MODE == 1 ? 5 : 4;
#define XST(i)
  const int l = tid & 63, hh = l >> 5, r = l & 31, rb = w & 3, nh = w >> 2;
  const uint32_t xvo = (8 * w + (l >> 3)) * 1280 + (((l & 7) ^ ((4 * w + (l >> 4)) & 7)) << 4);
  const uint32_t xso = rg * 128 * 1280;
  const uint32_t wk = w * 1024;
#define ISS_X(S, H) dma16(((2 * (S)) & 7) * SLOT + (H) * 8192 + wk, xvo, rx, xso + (S) * 128 + (H) * 64 * 1280)
#define ISS_W(S, H) dma16(((2 * (S) + 1) & 7) * SLOT + (H) * 8192 + wk, (H) == 0 ? v0 : (H) == 1 ? v1 : v2, MODE ? rq : (H) == 0 ? rg_ : ru_, fso + (S) * FSTR)
#define ISS_WW(S) do { ISS_W(S, 0); ISS_W(S, 1); if (MODE == 1) ISS_W(S, 2); } while (0)
#define ISS_N(J, H) dma16(((4 + (J)) % 6) * SLOT + (H) * 8192 + wk, guvo, (H) ? ru_ : rg_, nso + (J) * 64 * 3072)
  ISS_X(0, 0); ISS_X(0, 1);
  const uint32_t guvo = (8 * w + (l >> 3)) * 3072 + (((l & 7) ^ (((l >> 4) & 1) << 2)) << 4);
  const uint32_t v0 = MODE ? kvq_voff(w, l, ci, false) : guvo, v1 = MODE ? kvq_voff(w + 8, l, ci, false) : guvo;
  const uint32_t v2 = kvq_voff(rb + 16, l, ci, false);
  const uint32_t fso = MODE ? 0 : ci * 128, nso = (MODE ? 16 + ci : 8 + ci) * 128;
  constexpr uint32_t FSTR = MODE ? 64 * 3840 : 64 * 3072;
  ISS_WW(0);
  u32x2 eld = aload64(rws, 0, WS_E);
  const int strip = b >> 1, tok = rg * 128 + ((MODE ? 8 : 0) + ci) * 8 + w;
  const uint32_t sstr = strip < 30 ? 3840 : 3072, scol = strip < 30 ? strip : strip < 54 ? strip - 30 : strip - 54;
  const rsrc_t rsj = strip < 30 ? rq : strip < 54 ? rg_ : ru_;
  const uint32_t sjv = b < 156 ? (l >> 3) * sstr + (l & 7) * 16 : OOB;
  u32x4 sj[5], sx0 = aload128p(rx, l * 16, tok * 1280), sx1 = aload128p(rx, l < 16 ? l * 16 + 1024 : OOB, tok * 1280);
#pragma unroll
  for (int i = 0; i < 5; ++i) sj[i] = aload128p(rsj, sjv, ((b & 1) * 320 + w * 40 + i * 8) * sstr + scol * 128);
  ISS_X(1, 0); ISS_X(1, 1); ISS_WW(1);
  const int tb = b < 240 ? b : b - 240, t0ks = tb / 5, t0cg = tb - 5 * t0ks;
  const uint32_t xra = (32 * rb + r) * 128 + ((hh ^ ((r >> 1) & 7)) << 4);
  const uint32_t wa = MODE ? (8 * hh + ((l >> 2) & 3)) * 320 + ((l >> 4) & 1) * 32 + 8 * (l & 3) + (MODE - 1) * 192
                           : (8 * hh + ((l >> 2) & 3)) * 128 + 64 * (nh ^ ((l >> 3) & 1)) + 32 * ((l >> 4) & 1) + 8 * (l & 3);
  constexpr int KST = MODE ? 5120 : 2048, HO = MODE ? 1280 : 512, B1 = MODE ? 64 : 8192;
  const uint32_t CO0 = WS_CSG + (MODE == 0 ? 1920 + 64 * ci + 32 * nh : MODE == 1 ? 640 + 80 * ci : 1296 + 80 * ci) * 16;
  const uint32_t CO1 = CO0 + (MODE == 0 ? 1536 : 32) * 16;
  f32x16 a0 = {}, a1 = {}, a2 = {};
  bf16x8 w0, w1, w2;
  u32x4 gs; u32x2 g0, g1, g2;
  XST(11); XST(12);
  open_stage<D + 8>();
  STAMP(1);
  lptr base = lp(SLOT + wa);
  xf[0] = lds_r128((lptr)xra);
  w0 = trd2(base, base + HO); w1 = trd2(base + B1, base + B1 + HO);
  if (MODE == 1) w2 = trd2(base + 128, base + 128 + HO);
#pragma unroll
  for (int t = 1; t < 4; ++t) xf[t] = lds_r128((lptr)(xra ^ (t << 5)));
#define MM(A, W) A = MODE ? MFMA32(asbf(xf[ks]), W, A) : MFMA32(W, asbf(xf[ks]), A)
#pragma unroll
  for (int s = 0; s < 10; ++s) {
#pragma unroll
    for (int t = 0; t < 4; ++t) {
      const int ks = 4 * s + t;
      lptr o = base + (t + 1) * KST;
      uint32_t xb = 0;
      SB0();
      pin<true>(ks, xf[ks]);
      MM(a0, w0);
      if (MODE == 1 && t == 3) MM(a1, w1);
      SB0();
      if (t == 3 && s < 9) {
        if (s == 0 || s == 2 || s == 3) open_stage<2 * D + 2>();
        else if (s == 1) open_stage<2 * D + 3>();
        else if (s == 6) open_stage<2 * D>();
        else if (s == 7) open_stage<D + 4>();
        else if (s == 8) open_stage<D + 3>();
        else open_stage<2 * D + 1>();
        XST(13 + s);
        o = base = lp(((2 * s + 3) & 7) * SLOT + wa);
        xb = opq(((2 * s + 2) & 7) * SLOT + xra);
        xf[ks + 1] = lds_r128((lptr)xb);
      }
      if (ks < 39) w0 = trd2(o, o + HO);
      if (s == 0 && t < 2) { ISS_X(2 + t, 0); ISS_X(2 + t, 1); }
      if (s >= 1 && s <= 6) { if (t == 0) ISS_X(s + 3, 0); if (t == 1) ISS_W(s + 3, 0); if (t == 2 && MODE == 1) ISS_W(s + 3, 2); }
      if ((s == 7 || s == 9) && t < 2) ISS_N(s - 7 + t, 0);
      SB0();
      if (MODE != 1 || t != 3) MM(a1, w1);
      SB0();
      if (ks < 39) w1 = trd2(o + B1, o + B1 + HO);
      if (s == 0 && t < 2) ISS_WW(2 + t);
      if (s >= 1 && s <= 6) { if (t == 0) ISS_X(s + 3, 1); if (t == 1) ISS_W(s + 3, 1); }
      if ((s == 7 || s == 9) && t < 2) ISS_N(s - 7 + t, 1);
      if (MODE == 1) { SB0(); MM(a2, w2); SB0(); if (ks < 39) w2 = trd2(o + 128, o + 128 + HO); }
      if (t == 3 && s < 9) {
#pragma unroll
        for (int u = 1; u < 4; ++u) xf[ks + 1 + u] = lds_r128((lptr)(xb ^ (u << 5)));
      }
      if (s == 0 && t == 1)
        dma16(nh * 2 * SLOT + 16384 + rb * 1024, (4 * w + (l >> 4)) * 1280 + (l & 15) * 16, mk_rsrc(gwd, 1536 * 1280), t0ks * 32 * 1280 + t0cg * 256);
      if (s == 0 && t == 2) {
        wait_vm<3 * D + 1>();
        fence_reg(eld); fence_reg(sx0); fence_reg(sx1);
        const u64 e = ((u64)(uint32_t)__builtin_amdgcn_readfirstlane(eld[1]) << 32) | (uint32_t)__builtin_amdgcn_readfirstlane(eld[0]);
        tag = (e * 6364136223846793005ull + 1442695040888963407ull);
        u32x4 idn;
#pragma unroll
        for (int q = 0; q < 4; ++q) idn[q] = ((l & 7) >> 1) == q ? ((l & 1) ? 0x3f800000u : 0x3f80u) : 0u;
        f32x4 d = {};
#pragma unroll
        for (int i = 0; i < 5; ++i) { fence_reg(sj[i]); d = MFMA16(asbf(idn), asbf(sj[i]), d); }
        float sm = 0.f, sq = 0.f;
#pragma unroll
        for (int q = 0; q < 8; ++q) {
          const uint32_t u = q < 4 ? sx0[q & 3] : sx1[q & 3];
          const float lo = blo(u), hi = bhi(u);
          sm += lo + hi; sq = fmaf(lo, lo, fmaf(hi, hi, sq));
        }
        lds_w128((lptr)(SJA + w * 512 + (l & 15) * 32 + ((l >> 4) & 1) * 16), __builtin_bit_cast(u32x4, d));
        sm = wave_sum63(sm) * (1.f / 640.f); sq = wave_sum63(sq);
        const float ri = __builtin_amdgcn_rsqf(fmaxf(sq * (1.f / 640.f) - sm * sm, 0.f) + 1e-5f);
        const uint32_t t32 = tag >> 32;
        const u32x4 og = {__builtin_bit_cast(uint32_t, sm), t32, __builtin_bit_cast(uint32_t, ri), t32};
        st128_sc1(og, rws, l == 63 ? 0u : OOB, WS_STG + tok * 16);
      }
      if (s == 1 && t == 2) {
        const lptr a = lp(SJA + l * 4);
        float v = 0.f;
#pragma unroll
        for (int j = 0; j < 16; ++j) v += lds_rf(a + j * 256);
        const u32x2 og = {__builtin_bit_cast(uint32_t, v), (uint32_t)(tag >> 32)};
        __builtin_amdgcn_raw_buffer_store_b64(og, rws, (w == 7 && b < 156) ? l * 16 : OOB, WS_CSG + strip * 1024 + (b & 1) * 8, 16);
      }
      if (s == 3 && t == 2) {
        const int g = l >> 4;
        const lptr a = lp((g >> 1) * 2 * SLOT + 16384 + (4 * (g & 1) + ((l >> 2) & 3)) * 256 + w * 32 + 8 * (l & 3));
        const bf16x8 f = trd2(a, a + 8 * 256);
        const int cb = 8 * t0cg + w, c5 = cb / 5, n5 = cb - 5 * c5;
        st128_sc1(__builtin_bit_cast(u32x4, f), rws, l * 16, WS_W2 + ((c5 * 68 + t0ks) * 5 + n5) * 1024);
      }
      if (MODE != 2 && s == 7 && t == 2) publish_if0(w, rws, WS_FT + b * 8, (tag & ~0xffull) | 1);
      if (s == 8 && t == 0) {
        const int l3 = opq(tid) & 63;
        gs = aload128(rws, (l3 & 31) * 16, WS_STG + (rg * 128 + rb * 32) * 16);
        const uint32_t cv = (l3 & 31) * 16 + (l3 >> 5) * 8;
        g0 = aload64(rws, cv, CO0); g1 = aload64(rws, cv, CO1);
        if (MODE == 1) g2 = aload64(rws, cv + ((l3 & 16) ? 560 * 16 : 0), CO0 + 1024);
      }
    }
  }
  SB0();
  STAMP(2);
  const int l2 = opq(tid) & 63, r2 = l2 & 31;
  wait_vm<4>();
  fence_reg(gs); fence_reg(g0); fence_reg(g1); if (MODE == 1) fence_reg(g2);
  u32x2 sa = {gs[0], gs[1]}, sb = {gs[2], gs[3]};
  const uint32_t t32 = tag >> 32;
  if (__builtin_expect(!__all(sa[1] == t32 && sb[1] == t32 && g0[1] == t32 && g1[1] == t32 && (MODE != 1 || g2[1] == t32)), 0)) {
    const uint32_t so = WS_STG + (rg * 128 + rb * 32 + r2) * 16, cv = r2 * 16 + (l2 >> 5) * 8;
    gran_fix(sa, ws, so, t32); gran_fix(sb, ws, so + 8, t32); gran_fix(g0, ws, CO0 + cv, t32); gran_fix(g1, ws, CO1 + cv, t32);
    if (MODE == 1) gran_fix(g2, ws, CO0 + 1024 + cv + ((l2 & 16) ? 560 * 16 : 0), t32);
  }
  const uint32_t um = sa[0], ur = sb[0], u0 = g0[0], u1 = g1[0], u2 = MODE == 1 ? g2[0] : 0u;
  mean = __builtin_bit_cast(float, um); rs = __builtin_bit_cast(float, ur);
  const float c0 = __builtin_bit_cast(float, u0), c1 = __builtin_bit_cast(float, u1), c2 = __builtin_bit_cast(float, u2);
  if (MODE == 0) {
    h_store(a0, a1, c0, c1, mean, rs, l2, rws, WS_A2 + ((rg * 8 + 2 * rb) * 68 + 2 * ci + nh) * 1024);
  } else {
    lds_wf((lptr)(RSV + w * 128 + r2 * 4), rs);
    const bf16x8 mf = split3(-mean, false);
    a0 = MFMA32(mf, split3(c0, true), a0); a1 = MFMA32(mf, split3(c1, true), a1);
    if (MODE == 1) a2 = MFMA32(mf, split3(c2, true), a2);
    const lptr ra = lp(RSV + w * 128 + (l2 >> 5) * 16);
#pragma unroll
    for (int g = 0; g < 4; ++g) {
      const f32x4 rv = lds_rf4(ra + 32 * g);
#pragma unroll
      for (int j = 4 * g; j < 4 * g + 4; ++j) {
        a0[j] *= rv[j & 3]; a1[j] *= rv[j & 3];
        if (MODE == 1) { a0[j] = phi(a0[j]); a1[j] = phi(a1[j]); const float v = a2[j] * rv[j & 3], p = phi(v); a2[j] = (l2 & 16) ? v : p; }
      }
    }
    const lptr e0 = lp(MISC + (((MODE - 1) * 12 + rb) * 2) * 1024 + l2 * 16);
    lds_w128(e0, pack8(a0, 0)); lds_w128(e0 + 1024, pack8(a0, 1));
    lds_w128(e0 + 8192, pack8(a1, 0)); lds_w128(e0 + 8192 + 1024, pack8(a1, 1));
    if (MODE == 1) { lds_w128(e0 + 16384, pack8(a2, 0)); lds_w128(e0 + 16384 + 1024, pack8(a2, 1)); }
  }
}
template <int NH>
DEV void q_chunk(u32x4 (&xf)[40], float mean, float rs, int tid, int w, int ci, int rg, int ct, uint32_t fb_row,
                 rsrc_t rws, char* ws, u64 tag) {
  const int l = tid & 63, rb = w & 3;
  const uint32_t ta = (8 * (l >> 5) + ((l >> 2) & 3)) * 320 + ((l >> 4) & 1) * 32 + 8 * (l & 3) + NH * 128;
  const uint32_t ZP = MISC, ZS = MISC + 5120, X3 = MISC + 5632;
  const uint32_t l16 = l * 16;
  uint32_t cur = 2 * SLOT, prv = SLOT;
  f32x16 a0 = {}, a1 = {};
  bf16x8 w0, w1, n0, n1;
  u32x2 g0, g1;
  const uint32_t cv = (NH ? (l & 15) : (l & 31)) * 16 + (l >> 5) * 8, CO0 = WS_CSG + (80 * ci + 64 * NH) * 16;
  open_stage<8>();
  const int zt = min(tid, 319), zr = (zt * 3277) >> 16;
  u32x4 zp = aload128(rws, zr * KVREC + (zt - 20 * zr) * 16, WS_KVP + ci * 16 * KVREC + 6400);
  lptr base = lp(cur + ta);
  w0 = trd2(base, base + 1280);
  if (NH == 0) w1 = trd2(base + 64, base + 64 + 1280);
#pragma unroll
  for (int s = 0; s < 5; ++s) {
#pragma unroll
    for (int t = 0; t < 8; ++t) {
      const int ks = 8 * s + t;
      SB0();
      if (t < 2 || (t == 2 && NH == 0))
        dma16_sc1(prv + (w + 8 * t) * 1024, l16, rws, WS_W2 + (ct * 340 + s * 20 + w + 8 * t) * 1024);
      if (s == 2 && t == 0) {
        if (NH == 0) publish_if0(w, rws, WS_FB + rg * 128 + ci * 8, (tag & ~0xffull) | 2);
        fence_reg(zp); lds_w128(lp(ZP + zt * 16), zp);
      }
      if (NH == 1 && s == 3 && t == 0) {
        const int zi = min(tid - 256, 79);
        const lptr za = lp(ZP + zi * 4);
        float z = 0.f;
#pragma unroll
        for (int j = 0; j < 16; ++j) z += lds_rf(za + j * 320);
        lds_wf(za + 5120, z);
      }
      if (s == 3 && t == 3) { g0 = aload64(rws, cv, CO0); if (NH == 0) g1 = aload64(rws, cv, CO0 + 512); }
      if (t < 7 || s < 4) {
        if (t == 7) {
          if (s == 1) open_stage<4>(); else open_stage<8>();
          prv = cur; cur = nxt(cur); base = lp(cur + ta);
        }
        const int tn = (t + 1) & 7;
        const lptr o = base + (tn & 3) * 5120 + (tn >> 2) * 160;
        n0 = trd2(o, o + 1280);
        if (NH == 0) n1 = trd2(o + 64, o + 64 + 1280);
      }
      SB0();
      pin<false>(ks, xf[ks]);
      a0 = MFMA32(w0, asbf(xf[ks]), a0);
      if (NH == 0) a1 = MFMA32(w1, asbf(xf[ks]), a1);
      w0 = n0; if (NH == 0) w1 = n1;
    }
  }
  SB0();
  STAMP(8);
  const int l2 = opq(tid) & 63, r = l2 & 31, hh = l2 >> 5;
  const uint32_t pv = (r >> 4) * (68 * 1024) + (hh * 16 + (r & 15)) * 16;
  const uint32_t qb = WS_A2 + ((rg * 8 + 2 * rb) * 68 + 48) * 1024 + ci * 5 * 512;
  {
    wait_vm<3 - NH>(); fence_reg(g0); if (NH == 0) fence_reg(g1);
    const uint32_t u0 = g0[0], u1 = NH ? 0u : g1[0];
    const bf16x8 mf = split3(-mean, false);
    a0 = MFMA32(split3(__builtin_bit_cast(float, u0), true), mf, a0);
    if (NH == 0) a1 = MFMA32(split3(__builtin_bit_cast(float, u1), true), mf, a1);
  }
  const lptr zs = lp(ZS + 16 * hh), x3 = lp(X3 + (32 * rb + r) * 4);
  if (NH == 0) {
    u32x2 pl = aload64(rws, (l2 & 7) * 8, fb_row);
    float gs[8];
#pragma unroll
    for (int g = 0; g < 8; ++g) {
      const f32x4 z = lds_rf4(zs + 32 * g);
      float acc = 0.f;
#pragma unroll
      for (int j = 0; j < 4; ++j) {
        float v = phi(rs * (g < 4 ? a0[4 * g + j] : a1[4 * (g - 4) + j]));
        if (g < 4) a0[4 * g + j] = v; else a1[4 * (g - 4) + j] = v;
        acc = fmaf(v, z[j], acc);
      }
      gs[g] = acc;
    }
    float p0 = gs[0] + gs[1] + (hh ? 0.f : gs[2]);
    float p1 = gs[3] + gs[4] + (hh ? gs[2] : 0.f);
    float p2 = gs[5] + gs[6] + (hh ? 0.f : gs[7]);
    if (hh) lds_wf(x3, gs[7]);
    p0 += __shfl_xor(p0, 32); p1 += __shfl_xor(p1, 32); p2 += __shfl_xor(p2, 32);
    wait_vm<0>(); fence_reg(pl);
    if (w == 0) check_or_wait(pl, ws, fb_row + (l2 & 7) * 8, 2, tag);
    bar();
    const float p3 = gs[7] + lds_rf(x3 + 512);
    const float r0 = 1.f / (p0 + 1e-6f), r1 = 1.f / (p1 + 1e-6f), r2 = 1.f / (p2 + 1e-6f), r3 = 1.f / (p3 + 1e-6f);
    const float sc[8] = {r0, r0, hh ? r1 : r0, r1, r1, r2, r2, hh ? r3 : r2};
#pragma unroll
    for (int j = 0; j < 16; ++j) { a0[j] = a0[j] * sc[j >> 2]; a1[j] = a1[j] * sc[4 + (j >> 2)]; }
    st128_sc1(pack8(a0, 0), rws, pv, qb); st128_sc1(pack8(a0, 1), rws, pv, qb + 512);
    st128_sc1(pack8(a1, 0), rws, pv, qb + 1024); st128_sc1(pack8(a1, 1), rws, pv, qb + 1536);
  } else {
    float p3 = 0.f;
#pragma unroll
    for (int g = 0; g < 2; ++g) {
      const f32x4 z = lds_rf4(zs + 256 + 32 * g);
#pragma unroll
      for (int j = 0; j < 4; ++j) { float v = phi(rs * a0[4 * g + j]); a0[4 * g + j] = v; p3 = fmaf(v, z[j], p3); }
    }
    p3 += __shfl_xor(p3, 32);
    if (hh) lds_wf(x3 + 512, p3);
    bar();
    p3 += lds_rf(x3);
    const float r3 = 1.f / (p3 + 1e-6f);
#pragma unroll
    for (int j = 0; j < 8; ++j) a0[j] = a0[j] * r3;
    st128_sc1(pack8(a0, 0), rws, pv, qb + 2048);
  }
}

__global__ __launch_bounds__(512, 2) void fused_kernel(const void* __restrict__ gx, const void* __restrict__ gwqkv,
                                                       const void* __restrict__ gwo, const void* __restrict__ gwg,
                                                       const void* __restrict__ gwu, const void* __restrict__ gwd,
                                                       void* __restrict__ gout, char* __restrict__ ws) {
  const int tid = threadIdx.x;
  const int w = __builtin_amdgcn_readfirstlane(tid >> 6);
  const int rb = w & 3, nh = w >> 2;
  const int b = blockIdx.x;
  const int xcd = b & 7, idx = b >> 3;
  const int rg = (xcd >> 2) * 8 + (idx >> 2);
  const bool iskv = (idx & 2) != 0;
  const int ci = 2 * (xcd & 3) + (idx & 1);
  const int tt = (xcd >> 1) * 8 + (idx >> 2), ct = (xcd & 1) * 4 + (idx & 3);

  const rsrc_t rx = mk_rsrc(gx, 2048 * 1280), rqkv = mk_rsrc(gwqkv, 640 * 3840);
  const rsrc_t rg_ = mk_rsrc(gwg, 640 * 3072), ru_ = mk_rsrc(gwu, 640 * 3072);
  const rsrc_t rws = mk_rsrc(ws, WS_USED);
  STAMP(0);

  u64 tag;
  u32x4 xf[40];
  float mean, rs;
  const uint32_t mode = ((idx >> 1) & 1) * (1 + nh);
  if (mode == 0) {
    first_chunk<0>(xf, mean, rs, tag, opq(tid), w, b, rg, ci, rx, rqkv, rg_, ru_, gwd, rws, ws);
  }
  if (opqs(mode) == 1) first_chunk<1>(xf, mean, rs, tag, opq(tid), w, b, rg, ci, rx, rqkv, rg_, ru_, gwd, rws, ws);
  if (opqs(mode) == 2) first_chunk<2>(xf, mean, rs, tag, opq(tid), w, b, rg, ci, rx, rqkv, rg_, ru_, gwd, rws, ws);
  if (iskv) {
    const int t2 = opq(tid), l = t2 & 63, hh = l >> 5, r = l & 31;
    bar();
    const int kb = w < 5 ? (w >> 1) : (w - 5), vb = 2 + ((w + 1) >> 1);
    const lptr ka = lp(MISC + kb * 8192 + l * 16), va = lp(MISC + vb * 8192 + l * 16);
    f32x16 d = {};
    const uint32_t ck = 3 * SLOT;
    if (w < 5) {
#pragma unroll
      for (int j = 0; j < 8; ++j) d = MFMA32(asbf(lds_r128(ka + j * 1024)), asbf(lds_r128(va + j * 1024)), d);
      const int ve = 32 * vb + r - 80;
      const int hv = (ve * 205) >> 12;
      const int e = ve - 20 * hv;
      const int lo = 20 * hv - 32 * kb - 4 * hh;
      const uint32_t ca = ck + (hv * 400 + e - lo * 20) * 4;
#pragma unroll
      for (int j = 0; j < 16; ++j) {
        const int rowb = (j & 3) + 8 * (j >> 2);
        if (ve >= 0 && (unsigned)(rowb - lo) < 20u) lds_wf((lptr)(ca + rowb * 80), d[j]);
      }
    } else {
      const u32x4 ones = {0x3f803f80u, 0x3f803f80u, 0x3f803f80u, 0x3f803f80u};
#pragma unroll
      for (int j = 0; j < 8; ++j) d = MFMA32(asbf(ones), asbf(lds_r128(ka + j * 1024)), d);
      if (hh == 0 && 32 * kb + r < 80) lds_wf((lptr)(ck + 6400 + (32 * kb + r) * 4), d[0]);
    }
    bar();
    if (t2 < 420) st128_sc1(lds_r128((lptr)(ck + t2 * 16)), rws, t2 * 16, WS_KVP + (ci * 16 + rg) * KVREC);
  }
  STAMP(3);

  {
    const int l = opq(tid) & 63;
    const uint32_t ga = (8 * (l >> 5) + ((l >> 2) & 3)) * 128 + 64 * (nh ^ ((l >> 3) & 1)) + 32 * ((l >> 4) & 1) + 8 * (l & 3);
    const uint32_t guvo = (8 * w + (l >> 3)) * 3072 + (((l & 7) ^ (((l >> 4) & 1) << 2)) << 4);
    const int c = iskv ? 16 + ci : 8 + ci;
    uint32_t nv0, nv1, nv2, nso, nstr; rsrc_t nr0;
    if (!iskv) { nv0 = kvq_voff(w, l, ci, true); nv1 = kvq_voff(w + 8, l, ci, true); nv2 = kvq_voff((w & 3) + 16, l, ci, true);
                 nr0 = rqkv; nso = 0; nstr = 128 * 3840; }
    else { nv0 = l * 16; nv1 = l * 16 + 8192; nv2 = l * 16 + 16384; nr0 = rws; nso = WS_W2 + (ct * 340 + w) * 1024; nstr = 20480; }
    nso = __builtin_amdgcn_readfirstlane(nso); nstr = __builtin_amdgcn_readfirstlane(nstr);
    const uint32_t third = nh ^ 1, wk = w * 1024;
    const uint32_t gso = opqs(c * 128);
    f32x16 ag = {}, au = {};
    bf16x8 w0, w1;
    u32x2 g0, g1, fl;
    const u64 tg1 = (tag & ~0xffull) | 1;
    open_stage<6>();
    lptr base = lp(4 * SLOT + ga);
    w0 = trd2(base, base + 512); w1 = trd2(base + 8192, base + 8192 + 512);
#pragma unroll
    for (int s = 0; s < 10; ++s) {
#pragma unroll
      for (int t = 0; t < 4; ++t) {
        const int ks = 4 * s + t;
        const uint32_t prv = ((3 + s) % 6) * SLOT + wk;
        lptr o = base + (t + 1) * 2048;
        SB0();
        pin<true>(ks, xf[ks]);
        ag = MFMA32(w0, asbf(xf[ks]), ag);
        SB0();
        if (t == 3 && s < 9) { open_stage<8>(); o = base = lp(((5 + s) % 6) * SLOT + ga); }
        if (ks < 39) w0 = trd2(o, o + 512);
        if (s == 0 && t == 0) dma16(2 * SLOT + wk, guvo, rg_, 4 * 64 * 3072 + gso);
        if (s < 5) {
          if (t == 1) dma16(prv, guvo, rg_, (s + 5) * 64 * 3072 + gso);
          if (t == 2) dma16(prv + 8192, guvo, ru_, (s + 5) * 64 * 3072 + gso);
        } else {
          if (t == 0) dma16_sc1(prv, nv0, nr0, nso);
          if (t == 1) dma16_sc1(prv + 8192, nv1, nr0, nso);
          if (t == 2) { dma16_if(third, prv + 16384, nv2, nr0, nso); nso += nstr; }
        }
        SB0();
        au = MFMA32(w1, asbf(xf[ks]), au);
        SB0();
        if (ks < 39) w1 = trd2(o + 8192, o + 8192 + 512);
        if (s == 0 && t == 0) dma16(2 * SLOT + 8192 + wk, guvo, ru_, 4 * 64 * 3072 + gso);
        if (s == 2 && t == 0) fl = aload64(rws, (l & 31) * 8, WS_FT + w * 256);
        if (s == 3 && t == 0) publish_if0((~idx & 2) | w, rws, WS_FA + (ci * 16 + rg) * 8, tg1);
        if (s == 4 && t == 2) { wait_vm<6>(); acheck(fl, rws, (l & 31) * 8, WS_FT + w * 256, tg1); }
        if (s == 5 && t == 2) publish_if0(b | w, rws, WS_E, tag);
        if (s == 7 && t == 2) {
          const uint32_t cv = (l & 31) * 16 + (l >> 5) * 8, co = WS_CSG + (1920 + 64 * c + 32 * nh) * 16;
          g0 = aload64(rws, cv, co); g1 = aload64(rws, cv, co + 1536 * 16);
        }
        if (s == 8 && t == 2) fl = aload64(rws, (l & 15) * 8, WS_FA + ci * 128);
      }
    }
    SB0();
    STAMP(6);
    const int l2 = opq(tid) & 63;
    wait_vm<5>(); fence_reg(g0); fence_reg(g1);
    const uint32_t u0 = g0[0], u1 = g1[0];
    h_store(ag, au, __builtin_bit_cast(float, u0), __builtin_bit_cast(float, u1), mean, rs, l2, rws, WS_A2 + ((rg * 8 + 2 * rb) * 68 + 2 * c + nh) * 1024);
    wait_vm<4>(); acheck(fl, rws, (l2 & 15) * 8, WS_FA + ci * 128, tg1);
    STAMP(7);
  }
  uint32_t cur = 2 * SLOT, prv = SLOT;

  uint32_t pub_off, pub_lvl;
  const uint32_t fb_row = WS_FB + (tt >> 1) * 128;
  if (!iskv) {
    if (nh != 0) q_chunk<1>(xf, mean, rs, opq(tid), w, ci, rg, ct, fb_row, rws, ws, tag);
    if (opqs(nh) == 0) q_chunk<0>(xf, mean, rs, opq(tid), w, ci, rg, ct, fb_row, rws, ws, tag);
    cur = SLOT; prv = 0;
    pub_off = WS_FB + rg * 128 + ci * 8; pub_lvl = 3;
  } else {
    const int hl = rg & 3, qd = rg >> 2, head = 4 * ci + hl;
    const int tid = opq(threadIdx.x), l = tid & 63, q4 = (l >> 2) & 3, p4 = l & 3;
    const uint32_t WO = MISC, RED = MISC + 6400;
    bar();
    u32x2 pl; u32x4 wo4, k0, k1, k2, k3;
    if (w == 0) pl = aload64(rws, (l & 7) * 8, fb_row);
    if (tid < 400) {
      const int row = (tid * 3277) >> 16, rq = (tid * 656) >> 16;
      wo4 = aload128p(mk_rsrc(gwo, 640 * 1280), row * 1280 + (tid - 20 * row) * 16, head * 20 * 1280 + qd * 320);
      const uint32_t vo = rq * 4 * KVREC + (tid - 100 * rq) * 16, so = WS_KVP + ci * 16 * KVREC + hl * 1600;
      k0 = aload128(rws, vo, so); k1 = aload128(rws, vo, so + KVREC);
      k2 = aload128(rws, vo, so + 2 * KVREC); k3 = aload128(rws, vo, so + 3 * KVREC);
    }
    wait_vm<0>();
    if (tid < 400) {
      fence_reg(wo4); fence_reg(k0); fence_reg(k1); fence_reg(k2); fence_reg(k3);
      lds_w128((lptr)(WO + tid * 16), wo4);
      f32x4 sum = (__builtin_bit_cast(f32x4, k0) + __builtin_bit_cast(f32x4, k1)) + (__builtin_bit_cast(f32x4, k2) + __builtin_bit_cast(f32x4, k3));
      lds_w128((lptr)(RED + tid * 16), __builtin_bit_cast(u32x4, sum));
    }
    if (w == 0) { fence_reg(pl); check_or_wait(pl, ws, fb_row + (l & 7) * 8, 2, tag); }
    bar();
    if (tid == 0) flag_store(ws, WS_FB + rg * 128 + (8 + ci) * 8, (tag & ~0xffull) | 1);
    STAMP(8);
    {
      const int i16 = l & 15, g = l >> 4;
      u32x4 af[2];
#pragma unroll
      for (int db = 0; db < 2; ++db) {
        const int d = min(16 * db + i16, 19);
        f32x4 lo = {}, hi = {};
        const lptr al = lp(RED + d * 80 + min(g, 2) * 32), ah = lp(RED + d * 80 + min(g, 2) * 32 + (g < 2 ? 16 : 0));
#pragma unroll
        for (int j = 0; j < 4; ++j) {
          lo += lds_rf4(al + j * 1600);
          hi += lds_rf4(ah + j * 1600);
        }
        af[db][0] = g < 3 ? pk2(lo[0], lo[1]) : 0u; af[db][1] = g < 3 ? pk2(lo[2], lo[3]) : 0u;
        af[db][2] = g < 2 ? pk2(hi[0], hi[1]) : 0u; af[db][3] = g < 2 ? pk2(hi[2], hi[3]) : 0u;
      }
      const uint32_t b0 = WO + min(8 * g + q4, 16 + q4) * 320 + 8 * p4, b1 = WO + min(8 * g + 4 + q4, 16 + q4) * 320 + 8 * p4;
      for (int n = w; n < 10; n += 8) {
        const bf16x8 bf = trd2((lptr)(b0 + n * 32), (lptr)(b1 + n * 32));
        const f32x4 z4 = {};
        const f32x4 m0 = MFMA16(asbf(af[0]), bf, z4), m1 = MFMA16(asbf(af[1]), bf, z4);
        const int cb = 10 * qd + n, c5 = cb / 5, n5 = cb - 5 * c5;
        const uint32_t fb = WS_W2 + (c5 * 340 + 240 + n5) * 1024 + i16 * 16;
        const int G0 = 5 * head + g, G1 = 5 * head + 4;
        const uint32_t o0 = fb + (G0 >> 3) * 5120 + (((G0 >> 2) & 1) * 2 + (G0 & 1)) * 256 + ((G0 >> 1) & 1) * 8;
        const uint32_t o1 = fb + (G1 >> 3) * 5120 + (((G1 >> 2) & 1) * 2 + (G1 & 1)) * 256 + ((G1 >> 1) & 1) * 8;
        u32x2 v0 = {pk2(m0[0], m0[1]), pk2(m0[2], m0[3])}, v1 = {pk2(m1[0], m1[1]), pk2(m1[2], m1[3])};
        __builtin_amdgcn_raw_buffer_store_b64(v0, rws, o0, 0, 16);
        if (g == 0) __builtin_amdgcn_raw_buffer_store_b64(v1, rws, o1, 0, 16);
      }
    }
    pub_off = WS_FM + (qd * 32 + head) * 8; pub_lvl = 1;
  }
  STAMP(9);

  {
    const int kh = nh;
    const int tid = opq(threadIdx.x), l = tid & 63;
    u32x4 A[34];
    const int pr0 = (tid * 6554) >> 16;
    const int pr1 = ((tid + 512) * 6554) >> 16;
    const uint32_t xo0 = pr0 * 1280 + (tid - 10 * pr0) * 16, xo1 = pr1 * 1280 + (tid + 512 - 10 * pr1) * 16;
    const uint32_t xs = tt * 64 * 1280 + ct * 160;
    u32x4 xr0 = aload128p(rx, xo0, xs), xr1;
    if (tid < 128) xr1 = aload128p(rx, xo1, xs);
    const uint32_t as = WS_A2 + ((tt * 4 + rb) * 68 + 2 * kh) * 1024;
    const uint32_t l16 = l * 16;
#pragma unroll
    for (int p = 0; p < 5; ++p) { A[2 * p] = aload128(rws, l16, as + p * 4096); A[2 * p + 1] = aload128(rws, l16, as + p * 4096 + 1024); }
    u32x2 pl;
    const uint32_t po2 = fb_row + 64 + (l & 7) * 8;
    const uint32_t po3 = l < 32 ? WS_FM + (ct >> 1) * 256 + l * 8 : fb_row + (l & 7) * 8;
    if (w == 0) pl = aload64(rws, po2, 0);
    f32x4 acc[5] = {};
#pragma unroll
    for (int p = 0; p < 17; ++p) {
      const int lo = p < 5 ? 0 : p - 4, hi = p - 1 < 11 ? p - 1 : 11;
      const int nop = hi >= lo ? hi - lo + 1 : 0, ex = p < 5 ? 2 * (4 - p) : 0;
      if (p == 3 || p == 7) {
        if (w == 0) {
          wait_vm<15>(); fence_reg(pl);
          if (p == 3) check_or_wait(pl, ws, po2, 1, tag); else check_or_wait(pl, ws, po3, l < 32 ? 1 : 3, tag);
        }
      }
      switch (nop * 16 + ex) {
#define OPEN_CASE(NOP, EX) case NOP * 16 + EX: open_stage2<NOP * 5 + EX, NOP * 4 + EX>(nh); break;
        OPEN_CASE(0, 8) OPEN_CASE(1, 6) OPEN_CASE(2, 4) OPEN_CASE(3, 2) OPEN_CASE(4, 0)
        OPEN_CASE(3, 0) OPEN_CASE(2, 0) OPEN_CASE(1, 0) OPEN_CASE(0, 0)
        default: open_stage<0>();
      }
      fence_reg(A[2 * p]); fence_reg(A[2 * p + 1]);
      if (p == 0 && tid == 0) flag_store(ws, pub_off, (tag & ~0xffull) | pub_lvl);
      const lptr rbs = lp(cur + kh * 10240 + l16);
      u32x4 B[10];
#pragma unroll
      for (int j = 0; j < 10; ++j) B[j] = lds_r128(rbs + j * 1024);
      SB0();
      if (p + 5 < 17) {
        const uint32_t so = WS_W2 + (ct * 340 + (p + 5) * 20 + w) * 1024;
        dma16_sc1(prv + w * 1024, l16, rws, so);
        dma16_sc1(prv + (w + 8) * 1024, l16, rws, so + 8192);
        dma16_if(nh ^ 1, prv + (w + 16) * 1024, l16, rws, so + 16384);
        A[2 * p + 10] = aload128(rws, l16, as + (p + 5) * 4096);
        A[2 * p + 11] = aload128(rws, l16, as + (p + 5) * 4096 + 1024);
      }
      if (p == 3 && w == 0) pl = aload64(rws, po3, 0);
      SB0();
#pragma unroll
      for (int j = 0; j < 10; ++j) acc[j % 5] = MFMA16(asbf(B[j]), asbf(A[2 * p + j / 5]), acc[j % 5]);
      SB0();
      prv = cur; cur = nxt(cur);
    }
    STAMP(10);
    bar();
    const lptr ow = lp((kh * 64 + rb * 16 + (l & 15)) * 336 + (l >> 4) * 16);
#pragma unroll
    for (int j = 0; j < 5; ++j) lds_w128(ow + j * 64, __builtin_bit_cast(u32x4, acc[j]));
    bar();
    const rsrc_t ro = mk_rsrc(gout, 2048 * 1280);
#pragma unroll
    for (int h = 0; h < 2; ++h) {
      if (h == 0 || tid < 128) {
        u32x4 xr = h ? xr1 : xr0;
        fence_reg(xr);
        const int pr = h ? pr1 : pr0, pc = tid + 512 * h - 10 * pr;
        const lptr a = lp(pr * 336 + pc * 32);
        f32x4 u0 = lds_rf4(a) + lds_rf4(a + 64 * 336);
        f32x4 u1 = lds_rf4(a + 16) + lds_rf4(a + 16 + 64 * 336);
        u32x4 o;
        o[0] = pk2(u0[0] + blo(xr[0]), u0[1] + bhi(xr[0])); o[1] = pk2(u0[2] + blo(xr[1]), u0[3] + bhi(xr[1]));
        o[2] = pk2(u1[0] + blo(xr[2]), u1[1] + bhi(xr[2])); o[3] = pk2(u1[2] + blo(xr[3]), u1[3] + bhi(xr[3]));
        __builtin_amdgcn_raw_buffer_store_b128(o, ro, h ? xo1 : xo0, xs, 0);
      }
    }
    STAMP(11);
  }
}

extern "C" void kernel_launch(void* const* d_in, const int* in_sizes, int n_in, void* d_out, int out_size, void* d_ws,
                              size_t ws_size, hipStream_t stream) {
  (void)in_sizes; (void)n_in; (void)out_size; (void)ws_size;
  (void)hipFuncSetAttribute((const void*)fused_kernel, hipFuncAttributeMaxDynamicSharedMemorySize, LDS_BYTES);
  fused_kernel<<<256, 512, LDS_BYTES, stream>>>(d_in[0], d_in[1], d_in[2], d_in[4], d_in[5], d_in[6], d_out, (char*)d_ws);
}
```

```cpp
#include <hip/hip_runtime.h>
#include <stdint.h>

typedef __attribute__((ext_vector_type(8))) __bf16 bf16x8;
typedef __attribute__((ext_vector_type(4))) __bf16 bf16x4;
typedef __attribute__((ext_vector_type(2))) __bf16 bf16x2;
typedef __attribute__((ext_vector_type(16))) float f32x16;
typedef __attribute__((ext_vector_type(4))) float f32x4;
typedef __attribute__((ext_vector_type(2))) float f32x2;
typedef __attribute__((ext_vector_type(4))) unsigned u32x4;
typedef __attribute__((ext_vector_type(2))) unsigned u32x2;
typedef __amdgpu_buffer_rsrc_t rsrc_t;
typedef unsigned long long u64;
#define AS3 __attribute__((address_space(3)))
#define AS1 __attribute__((address_space(1)))
#define DEV __device__ __forceinline__
#define SB0() __builtin_amdgcn_sched_barrier(0)

constexpr uint32_t SLOT = 20480, RING = 122880, MISC = RING, LDS_BYTES = 163840;
constexpr uint32_t WS_E = 0, WS_FT = 4096, WS_FA = 8192, WS_FB = 12288, WS_FM = 16384;
constexpr uint32_t WS_KVP = 32768, KVREC = 6912;
constexpr uint32_t WS_W2 = 1u << 20, WS_A2 = 4u << 20;
constexpr uint32_t WS_USED = WS_A2 + 128u * 68u * 1024u;
constexpr uint32_t SPIN_CAP = 1u << 16;

DEV rsrc_t mk_rsrc(const void* p, uint32_t bytes) {
  return __builtin_amdgcn_make_buffer_rsrc((void*)p, 0, bytes, 0x00020000);
}
DEV void dma16(uint32_t m0v, uint32_t voff, rsrc_t srd, uint32_t soff) {
  asm volatile("s_mov_b32 m0, %0\n\ts_nop 0\n\tbuffer_load_dwordx4 %1, %2, %3 offen lds"
               :: "s"(m0v), "v"(voff), "s"(srd), "s"(soff) : "memory");
}
DEV void dma16_sc1(uint32_t m0v, uint32_t voff, rsrc_t srd, uint32_t soff) {
  asm volatile("s_mov_b32 m0, %0\n\ts_nop 0\n\tbuffer_load_dwordx4 %1, %2, %3 offen sc1 lds"
               :: "s"(m0v), "v"(voff), "s"(srd), "s"(soff) : "memory");
}
DEV void dma16_if(uint32_t cond, uint32_t m0v, uint32_t voff, rsrc_t srd, uint32_t soff) {
  asm volatile("s_cmp_eq_u32 %4, 0\n\ts_cbranch_scc1 1f\n\ts_mov_b32 m0, %0\n\ts_nop 0\n\t"
               "buffer_load_dwordx4 %1, %2, %3 offen sc1 lds\n1:"
               :: "s"(m0v), "v"(voff), "s"(srd), "s"(soff), "s"(cond) : "memory", "scc");
}
DEV void dma8_sc1(uint32_t m0v, uint32_t voff, rsrc_t srd, uint32_t soff) {
  asm volatile("s_mov_b32 m0, %0\n\ts_mov_b32 exec_hi, 0\n\tbuffer_load_dwordx4 %1, %2, %3 offen sc1 lds\n\ts_mov_b32 exec_hi, -1"
               :: "s"(m0v), "v"(voff), "s"(srd), "s"(soff) : "memory");
}
template <int N> DEV void open_nl() { asm volatile("s_waitcnt vmcnt(%0)\n\ts_barrier" :: "n"(N) : "memory"); }
template <int N> DEV void wait_vm() { asm volatile("s_waitcnt vmcnt(%0)" :: "n"(N) : "memory"); }
DEV void bar() { asm volatile("s_waitcnt lgkmcnt(0)\n\ts_barrier" ::: "memory"); }
template <int N> DEV void open_stage() {
  asm volatile("s_waitcnt vmcnt(%0) lgkmcnt(0)\n\ts_barrier" :: "n"(N) : "memory");
}
template <int N0, int N1> DEV void open_stage2(uint32_t sel) {
  asm volatile("s_cmp_eq_u32 %2, 0\n\ts_cbranch_scc1 1f\n\ts_waitcnt vmcnt(%1)\n1:\n\ts_waitcnt vmcnt(%0) lgkmcnt(0)\n\ts_barrier"
               :: "n"(N0), "n"(N1), "s"(sel) : "memory", "scc");
}
template <int N0, int N1> DEV void wait_vm2(uint32_t sel) {
  asm volatile("s_cmp_eq_u32 %2, 0\n\ts_cbranch_scc1 1f\n\ts_waitcnt vmcnt(%1)\n1:\n\ts_waitcnt vmcnt(%0)" :: "n"(N0), "n"(N1), "s"(sel) : "memory", "scc");
}
DEV void publish_if0(uint32_t cond, rsrc_t r, uint32_t soff, u64 val) {
  u64 t;
  asm volatile("s_cmp_lg_u32 %4, 0\n\ts_cbranch_scc1 1f\n\tv_mov_b64 %0, %1\n\ts_mov_b64 exec, 1\n\t"
               "buffer_store_dwordx2 %0, off, %2, %3 sc1\n\ts_mov_b64 exec, -1\n1:"
               : "=&v"(t) : "s"(val), "s"(r), "s"(soff), "s"(cond) : "memory", "scc");
}
typedef AS3 char* lptr;
DEV lptr lp(uint32_t a) { asm volatile("" : "+v"(a)); return (lptr)a; }
DEV bf16x4 trd(lptr a) { return __builtin_amdgcn_ds_read_tr16_b64_v4bf16((AS3 bf16x4*)a); }
DEV bf16x8 trd2(lptr a0, lptr a1) {
  bf16x4 lo = trd(a0), hi = trd(a1);
  return __builtin_shufflevector(lo, hi, 0, 1, 2, 3, 4, 5, 6, 7);
}
DEV u32x4 lds_r128(lptr a) { return *(AS3 u32x4*)a; }
DEV f32x4 lds_rf4(lptr a) { return *(AS3 f32x4*)a; }
DEV void lds_w128(lptr a, u32x4 v) { *(AS3 u32x4*)a = v; }
DEV float lds_rf(lptr a) { return *(AS3 float*)a; }
DEV void lds_wf(lptr a, float v) { *(AS3 float*)a = v; }
DEV int opq(int v) { asm volatile("" : "+v"(v)); return v; }
DEV uint32_t opqs(uint32_t v) { asm volatile("" : "+s"(v)); return v; }
DEV uint32_t pk2(float a, float b) {
  f32x2 v = {a, b};
  return __builtin_bit_cast(uint32_t, __builtin_convertvector(v, bf16x2));
}
DEV float blo(uint32_t u) { return __builtin_bit_cast(float, u << 16); }
DEV float bhi(uint32_t u) { return __builtin_bit_cast(float, u & 0xffff0000u); }
DEV float phi(float t) {
  float e = __builtin_amdgcn_exp2f(t * 1.4426950408889634f);
  e = __builtin_amdgcn_fmed3f(e, 0.f, 1.f);
  return fmaxf(t, 0.f) + e + 1e-6f;
}
#define MFMA32(a, b, c) __builtin_amdgcn_mfma_f32_32x32x16_bf16(a, b, c, 0, 0, 0)
#define MFMA16(a, b, c) __builtin_amdgcn_mfma_f32_16x16x32_bf16(a, b, c, 0, 0, 0)
DEV bf16x8 asbf(u32x4 v) { return __builtin_bit_cast(bf16x8, v); }
DEV u32x4 pack8(const f32x16& a, int s) {
  u32x4 r;
  r[0] = pk2(a[8 * s + 0], a[8 * s + 1]); r[1] = pk2(a[8 * s + 2], a[8 * s + 3]);
  r[2] = pk2(a[8 * s + 4], a[8 * s + 5]); r[3] = pk2(a[8 * s + 6], a[8 * s + 7]);
  return r;
}
DEV void st128_sc1(u32x4 v, rsrc_t r, uint32_t voff, uint32_t soff) {
  asm volatile("buffer_store_dwordx4 %0, %1, %2, %3 offen sc1\n\ts_nop 1" :: "v"(v), "v"(voff), "s"(r), "s"(soff) : "memory");
}
DEV void flag_store(char* ws, uint32_t off, u64 v) {
  __hip_atomic_store((AS1 u64*)(ws + off), v, __ATOMIC_RELAXED, __HIP_MEMORY_SCOPE_AGENT);
}
DEV u64 flag_load(char* ws, uint32_t off) {
  return __hip_atomic_load((AS1 u64*)(ws + off), __ATOMIC_RELAXED, __HIP_MEMORY_SCOPE_AGENT);
}
DEV bool flag_ok(u64 v, u64 tag, uint32_t lvl) { return ((v ^ tag) >> 8) == 0 && (uint32_t)(v & 0xff) >= lvl; }
DEV u32x2 aload64(rsrc_t r, uint32_t voff, uint32_t soff) {
  u32x2 v; asm volatile("buffer_load_dwordx2 %0, %1, %2, %3 offen sc1" : "=v"(v) : "v"(voff), "s"(r), "s"(soff) : "memory"); return v;
}
DEV uint32_t aload32(rsrc_t r, uint32_t voff, uint32_t soff) {
  uint32_t v; asm volatile("buffer_load_dword %0, %1, %2, %3 offen sc1" : "=v"(v) : "v"(voff), "s"(r), "s"(soff) : "memory"); return v;
}
DEV u32x4 aload128(rsrc_t r, uint32_t voff, uint32_t soff) {
  u32x4 v; asm volatile("buffer_load_dwordx4 %0, %1, %2, %3 offen sc1" : "=v"(v) : "v"(voff), "s"(r), "s"(soff) : "memory"); return v;
}
DEV u32x4 aload128p(rsrc_t r, uint32_t voff, uint32_t soff) {
  u32x4 v; asm volatile("buffer_load_dwordx4 %0, %1, %2, %3 offen" : "=v"(v) : "v"(voff), "s"(r), "s"(soff) : "memory"); return v;
}
template <typename T> DEV void fence_reg(T& v) { asm volatile("" : "+v"(v) :: "memory"); }
DEV u64 u64of(u32x2 v) { return ((u64)v[1] << 32) | v[0]; }
DEV void poll_wait(char* ws, uint32_t off, uint32_t lvl, u64 tag) {
  for (uint32_t it = 0; it < SPIN_CAP; ++it) {
    u64 v = flag_load(ws, off);
    if (__all(flag_ok(v, tag, lvl))) break;
    __builtin_amdgcn_s_sleep(1);
  }
}
DEV void check_or_wait(u32x2 got, char* ws, uint32_t off, uint32_t lvl, u64 tag) {
  if (!__all(flag_ok(u64of(got), tag, lvl))) poll_wait(ws, off, lvl, tag);
}
#define STAMP(i) do {} while (0)
#define PINX(I, A, D) case I: if (RW) asm volatile("" : "+{v[" #A ":" #D "]}"(f)); else asm volatile("" :: "{v[" #A ":" #D "]}"(f)); break;
template <bool RW> DEV void pin(int i, u32x4& f) {
  switch (i) {
    PINX(0, 96, 99) PINX(1, 100, 103) PINX(2, 104, 107) PINX(3, 108, 111) PINX(4, 112, 115)
    PINX(5, 116, 119) PINX(6, 120, 123) PINX(7, 124, 127) PINX(8, 128, 131) PINX(9, 132, 135)
    PINX(10, 136, 139) PINX(11, 140, 143) PINX(12, 144, 147) PINX(13, 148, 151) PINX(14, 152, 155)
    PINX(15, 156, 159) PINX(16, 160, 163) PINX(17, 164, 167) PINX(18, 168, 171) PINX(19, 172, 175)
    PINX(20, 176, 179) PINX(21, 180, 183) PINX(22, 184, 187) PINX(23, 188, 191) PINX(24, 192, 195)
    PINX(25, 196, 199) PINX(26, 200, 203) PINX(27, 204, 207) PINX(28, 208, 211) PINX(29, 212, 215)
    PINX(30, 216, 219) PINX(31, 220, 223) PINX(32, 224, 227) PINX(33, 228, 231) PINX(34, 232, 235)
    PINX(35, 236, 239) PINX(36, 240, 243) PINX(37, 244, 247) PINX(38, 248, 251) PINX(39, 252, 255)
  }
}
DEV bf16x8 split3(float v, bool side) {
  const uint32_t h = __builtin_bit_cast(uint32_t, v);
  const float r1 = v - bhi(h);
  const uint32_t m = __builtin_bit_cast(uint32_t, r1);
  const uint32_t l = __builtin_bit_cast(uint32_t, r1 - bhi(m));
  u32x4 o;
#define PK(LO, HI) __builtin_amdgcn_perm(HI, LO, 0x07060302)
  if (side) { o[0] = PK(h, h); o[1] = PK(m, h); o[2] = PK(l, m); o[3] = PK(m, l); }
  else { o[0] = PK(h, m); o[1] = PK(h, l); o[2] = PK(h, m); o[3] = PK(l, m); }
  return asbf(o);
}
DEV float sum4(f32x4 v) { return (v[0] + v[1]) + (v[2] + v[3]); }
DEV uint32_t nxt(uint32_t s) { return s + SLOT == RING ? 0u : s + SLOT; }
constexpr uint32_t SJA = 4 * SLOT + 16384, RSV = 16384;
constexpr uint32_t WS_CSG = 917504, WS_STG = 997376, OOB = 0x7ffffff0u;
template <int CTRL> DEV float dpp_add(float v) {
  return v + __builtin_bit_cast(float, __builtin_amdgcn_update_dpp(0, __builtin_bit_cast(int, v), CTRL, 0xf, 0xf, true));
}
DEV float wave_sum63(float v) {
  v = dpp_add<0x111>(v); v = dpp_add<0x112>(v); v = dpp_add<0x114>(v); v = dpp_add<0x118>(v);
  v += __builtin_bit_cast(float, __builtin_amdgcn_update_dpp(0, __builtin_bit_cast(int, v), 0x142, 0xa, 0xf, true));
  v += __builtin_bit_cast(float, __builtin_amdgcn_update_dpp(0, __builtin_bit_cast(int, v), 0x143, 0xc, 0xf, true));
  return v;
}
DEV void gran_fix(u32x2& g, char* ws, uint32_t off, uint32_t t32) {
  for (uint32_t it = 0; it < SPIN_CAP && !__all(g[1] == t32); ++it) {
    const u64 v = flag_load(ws, off);
    g[0] = (uint32_t)v; g[1] = (uint32_t)(v >> 32);
  }
}

DEV uint32_t kvq_voff(int piece, int lane, int i, bool isq) {
  uint32_t p = piece * 1024 + lane * 16;
  uint32_t row = (p * 52429u) >> 24;
  uint32_t col = p - row * 320;
  bool hi = col >= 160;
  uint32_t c = hi ? col - 160 : col;
  return isq ? (row + (hi ? 64u : 0u)) * 3840 + 160 * i + c
             : row * 3840 + (hi ? 2560u : 1280u) + 160 * i + c;
}
DEV void check4(u32x4 ft0, u32x4 ft1, char* ws, uint32_t off, u64 tag) {
  u32x2 f0 = {ft0[0], ft0[1]}, f1 = {ft0[2], ft0[3]}, f2 = {ft1[0], ft1[1]}, f3 = {ft1[2], ft1[3]};
  bool ok = flag_ok(u64of(f0), tag, 1) && flag_ok(u64of(f1), tag, 1) && flag_ok(u64of(f2), tag, 1) && flag_ok(u64of(f3), tag, 1);
  if (!__all(ok)) {
#pragma unroll 1
    for (int j = 0; j < 4; ++j) poll_wait(ws, off + 8 * j, 1, tag);
  }
}
DEV void h_store(f32x16& ag, f32x16& au, float cg, float cu, float mean, float rs, int tid, rsrc_t rws, uint32_t hs) {
  const bf16x8 mf = split3(-mean, false);
  ag = MFMA32(split3(cg, true), mf, ag);
  au = MFMA32(split3(cu, true), mf, au);
  const float r2 = rs * rs;
#pragma unroll
  for (int j = 0; j < 16; ++j) ag[j] = (ag[j] * r2) * au[j];
  const int l2 = tid & 63, r = l2 & 31, hh = l2 >> 5;
  const uint32_t hv = (r >> 4) * (68 * 1024) + (hh * 16 + (r & 15)) * 16;
  st128_sc1(pack8(ag, 0), rws, hv, hs);
  st128_sc1(pack8(ag, 1), rws, hv + 512, hs);
}

DEV void acheck(u32x2 f, rsrc_t r, uint32_t voff, uint32_t soff, u64 lo) {
  u64 m; uint32_t n;
  asm volatile("s_mov_b32 %2, 0x10000\n\ts_branch 2f\n1:\n\tbuffer_load_dwordx2 %0, %3, %4, %5 offen sc1\n\ts_waitcnt vmcnt(0)\n2:\n\t"
               "v_cmp_gt_u64 vcc, %6, %0\n\tv_cmp_lt_u64 %1, %7, %0\n\ts_sub_u32 %2, %2, 1\n\ts_cselect_b64 %1, 0, %1\n\ts_cselect_b64 vcc, 0, vcc\n\t"
               "s_or_b64 %1, %1, vcc\n\ts_cbranch_scc1 1b"
               : "+v"(f), "=&s"(m), "=&s"(n) : "v"(voff), "s"(r), "s"(soff), "s"(lo), "s"(lo | 0xff) : "memory", "vcc", "scc");
}
template <int MODE>
DEV void first_chunk(u32x4 (&xf)[40], float& mean, float& rs, u64& tag, int tid, int w, int b, int rg, int ci, rsrc_t rx,
                     rsrc_t rq, rsrc_t rg_, rsrc_t ru_, const void* gwd, rsrc_t rws, char* ws) {
  constexpr int D = MODE == 1 ? 5 : 4;
#define XST(i)
  const int l = tid & 63, hh = l >> 5, r = l & 31, rb = w & 3, nh = w >> 2;
  const uint32_t xvo = (8 * w + (l >> 3)) * 1280 + (((l & 7) ^ ((4 * w + (l >> 4)) & 7)) << 4);
  const uint32_t xso = rg * 128 * 1280;
  const uint32_t wk = w * 1024;
#define ISS_X(S, H) dma16(((2 * (S)) & 7) * SLOT + (H) * 8192 + wk, xvo, rx, xso + (S) * 128 + (H) * 64 * 1280)
#define ISS_W(S, H) dma16(((2 * (S) + 1) & 7) * SLOT + (H) * 8192 + wk, (H) == 0 ? v0 : (H) == 1 ? v1 : v2, MODE ? rq : (H) == 0 ? rg_ : ru_, fso + (S) * FSTR)
#define ISS_WW(S) do { ISS_W(S, 0); ISS_W(S, 1); if (MODE == 1) ISS_W(S, 2); } while (0)
#define ISS_N(J, H) dma16(((4 + (J)) % 6) * SLOT + (H) * 8192 + wk, guvo, (H) ? ru_ : rg_, nso + (J) * 64 * 3072)
  ISS_X(0, 0); ISS_X(0, 1);
  const uint32_t guvo = (8 * w + (l >> 3)) * 3072 + (((l & 7) ^ (((l >> 4) & 1) << 2)) << 4);
  const uint32_t v0 = MODE ? kvq_voff(w, l, ci, false) : guvo, v1 = MODE ? kvq_voff(w + 8, l, ci, false) : guvo;
  const uint32_t v2 = kvq_voff(rb + 16, l, ci, false);
  const uint32_t fso = MODE ? 0 : ci * 128, nso = (MODE ? 16 + ci : 8 + ci) * 128;
  constexpr uint32_t FSTR = MODE ? 64 * 3840 : 64 * 3072;
  ISS_WW(0);
  u32x2 eld = aload64(rws, 0, WS_E);
  const int strip = b >> 1, tok = rg * 128 + ((MODE ? 8 : 0) + ci) * 8 + w;
  const uint32_t sstr = strip < 30 ? 3840 : 3072, scol = strip < 30 ? strip : strip < 54 ? strip - 30 : strip - 54;
  const rsrc_t rsj = strip < 30 ? rq : strip < 54 ? rg_ : ru_;
  const uint32_t sjv = b < 156 ? (l >> 3) * sstr + (l & 7) * 16 : OOB;
  u32x4 sj[5], sx0 = aload128p(rx, l * 16, tok * 1280), sx1 = aload128p(rx, l < 16 ? l * 16 + 1024 : OOB, tok * 1280);
#pragma unroll
  for (int i = 0; i < 5; ++i) sj[i] = aload128p(rsj, sjv, ((b & 1) * 320 + w * 40 + i * 8) * sstr + scol * 128);
  ISS_X(1, 0); ISS_X(1, 1); ISS_WW(1);
  const int tb = b < 240 ? b : b - 240, t0ks = tb / 5, t0cg = tb - 5 * t0ks;
  const uint32_t xra = (32 * rb + r) * 128 + ((hh ^ ((r >> 1) & 7)) << 4);
  const uint32_t wa = MODE ? (8 * hh + ((l >> 2) & 3)) * 320 + ((l >> 4) & 1) * 32 + 8 * (l & 3) + (MODE - 1) * 192
                           : (8 * hh + ((l >> 2) & 3)) * 128 + 64 * (nh ^ ((l >> 3) & 1)) + 32 * ((l >> 4) & 1) + 8 * (l & 3);
  constexpr int KST = MODE ? 5120 : 2048, HO = MODE ? 1280 : 512, B1 = MODE ? 64 : 8192;
  const uint32_t CO0 = WS_CSG + (MODE == 0 ? 1920 + 64 * ci + 32 * nh : MODE == 1 ? 640 + 80 * ci : 1296 + 80 * ci) * 16;
  const uint32_t CO1 = CO0 + (MODE == 0 ? 1536 : 32) * 16;
  f32x16 a0 = {}, a1 = {}, a2 = {};
  bf16x8 w0, w1, w2;
  u32x4 gs; u32x2 g0, g1, g2;
  XST(11); XST(12);
  open_stage<D + 8>();
  STAMP(1);
  lptr base = lp(SLOT + wa);
  xf[0] = lds_r128((lptr)xra);
  w0 = trd2(base, base + HO); w1 = trd2(base + B1, base + B1 + HO);
  if (MODE == 1) w2 = trd2(base + 128, base + 128 + HO);
#pragma unroll
  for (int t = 1; t < 4; ++t) xf[t] = lds_r128((lptr)(xra ^ (t << 5)));
#define MM(A, W) A = MODE ? MFMA32(asbf(xf[ks]), W, A) : MFMA32(W, asbf(xf[ks]), A)
#pragma unroll
  for (int s = 0; s < 10; ++s) {
#pragma unroll
    for (int t = 0; t < 4; ++t) {
      const int ks = 4 * s + t;
      lptr o = base + (t + 1) * KST;
      uint32_t xb = 0;
      SB0();
      pin<true>(ks, xf[ks]);
      MM(a0, w0);
      if (MODE == 1 && t == 3) MM(a1, w1);
      SB0();
      if (t == 3 && s < 9) {
        if (s == 0 || s == 2 || s == 3) open_stage<2 * D + 2>();
        else if (s == 1) open_stage<2 * D + 3>();
        else if (s == 6) open_stage<2 * D>();
        else if (s == 7) open_stage<D + 4>();
        else if (s == 8) open_stage<D + 3>();
        else open_stage<2 * D + 1>();
        XST(13 + s);
        o = base = lp(((2 * s + 3) & 7) * SLOT + wa);
        xb = opq(((2 * s + 2) & 7) * SLOT + xra);
        xf[ks + 1] = lds_r128((lptr)xb);
      }
      if (ks < 39) w0 = trd2(o, o + HO);
      if (s == 0 && t < 2) { ISS_X(2 + t, 0); ISS_X(2 + t, 1); }
      if (s >= 1 && s <= 6) { if (t == 0) ISS_X(s + 3, 0); if (t == 1) ISS_W(s + 3, 0); if (t == 2 && MODE == 1) ISS_W(s + 3, 2); }
      if ((s == 7 || s == 9) && t < 2) ISS_N(s - 7 + t, 0);
      SB0();
      if (MODE != 1 || t != 3) MM(a1, w1);
      SB0();
      if (ks < 39) w1 = trd2(o + B1, o + B1 + HO);
      if (s == 0 && t < 2) ISS_WW(2 + t);
      if (s >= 1 && s <= 6) { if (t == 0) ISS_X(s + 3, 1); if (t == 1) ISS_W(s + 3, 1); }
      if ((s == 7 || s == 9) && t < 2) ISS_N(s - 7 + t, 1);
      if (MODE == 1) { SB0(); MM(a2, w2); SB0(); if (ks < 39) w2 = trd2(o + 128, o + 128 + HO); }
      if (t == 3 && s < 9) {
#pragma unroll
        for (int u = 1; u < 4; ++u) xf[ks + 1 + u] = lds_r128((lptr)(xb ^ (u << 5)));
      }
      if (s == 0 && t == 1)
        dma16(nh * 2 * SLOT + 16384 + rb * 1024, (4 * w + (l >> 4)) * 1280 + (l & 15) * 16, mk_rsrc(gwd, 1536 * 1280), t0ks * 32 * 1280 + t0cg * 256);
      if (s == 0 && t == 2) {
        wait_vm<3 * D + 1>();
        fence_reg(eld); fence_reg(sx0); fence_reg(sx1);
        const u64 e = ((u64)(uint32_t)__builtin_amdgcn_readfirstlane(eld[1]) << 32) | (uint32_t)__builtin_amdgcn_readfirstlane(eld[0]);
        tag = (e * 6364136223846793005ull + 1442695040888963407ull);
        u32x4 idn;
#pragma unroll
        for (int q = 0; q < 4; ++q) idn[q] = ((l & 7) >> 1) == q ? ((l & 1) ? 0x3f800000u : 0x3f80u) : 0u;
        f32x4 d = {};
#pragma unroll
        for (int i = 0; i < 5; ++i) { fence_reg(sj[i]); d = MFMA16(asbf(idn), asbf(sj[i]), d); }
        float sm = 0.f, sq = 0.f;
#pragma unroll
        for (int q = 0; q < 8; ++q) {
          const uint32_t u = q < 4 ? sx0[q & 3] : sx1[q & 3];
          const float lo = blo(u), hi = bhi(u);
          sm += lo + hi; sq = fmaf(lo, lo, fmaf(hi, hi, sq));
        }
        lds_w128((lptr)(SJA + w * 512 + (l & 15) * 32 + ((l >> 4) & 1) * 16), __builtin_bit_cast(u32x4, d));
        sm = wave_sum63(sm) * (1.f / 640.f); sq = wave_sum63(sq);
        const float ri = __builtin_amdgcn_rsqf(fmaxf(sq * (1.f / 640.f) - sm * sm, 0.f) + 1e-5f);
        const uint32_t t32 = tag >> 32;
        const u32x4 og = {__builtin_bit_cast(uint32_t, sm), t32, __builtin_bit_cast(uint32_t, ri), t32};
        st128_sc1(og, rws, l == 63 ? 0u : OOB, WS_STG + tok * 16);
      }
      if (s == 1 && t == 2) {
        const lptr a = lp(SJA + l * 4);
        float v = 0.f;
#pragma unroll
        for (int j = 0; j < 16; ++j) v += lds_rf(a + j * 256);
        const u32x2 og = {__builtin_bit_cast(uint32_t, v), (uint32_t)(tag >> 32)};
        __builtin_amdgcn_raw_buffer_store_b64(og, rws, (w == 7 && b < 156) ? l * 16 : OOB, WS_CSG + strip * 1024 + (b & 1) * 8, 16);
      }
      if (s == 3 && t == 2) {
        const int g = l >> 4;
        const lptr a = lp((g >> 1) * 2 * SLOT + 16384 + (4 * (g & 1) + ((l >> 2) & 3)) * 256 + w * 32 + 8 * (l & 3));
        const bf16x8 f = trd2(a, a + 8 * 256);
        const int cb = 8 * t0cg + w, c5 = cb / 5, n5 = cb - 5 * c5;
        st128_sc1(__builtin_bit_cast(u32x4, f), rws, l * 16, WS_W2 + ((c5 * 68 + t0ks) * 5 + n5) * 1024);
      }
      if (MODE != 2 && s == 7 && t == 2) publish_if0(w, rws, WS_FT + b * 8, (tag & ~0xffull) | 1);
      if (s == 8 && t == 0) {
        const int l3 = opq(tid) & 63;
        gs = aload128(rws, (l3 & 31) * 16, WS_STG + (rg * 128 + rb * 32) * 16);
        const uint32_t cv = (l3 & 31) * 16 + (l3 >> 5) * 8;
        g0 = aload64(rws, cv, CO0); g1 = aload64(rws, cv, CO1);
        if (MODE == 1) g2 = aload64(rws, cv + ((l3 & 16) ? 560 * 16 : 0), CO0 + 1024);
      }
    }
  }
  SB0();
  STAMP(2);
  const int l2 = opq(tid) & 63, r2 = l2 & 31;
  wait_vm<4>();
  fence_reg(gs); fence_reg(g0); fence_reg(g1); if (MODE == 1) fence_reg(g2);
  u32x2 sa = {gs[0], gs[1]}, sb = {gs[2], gs[3]};
  const uint32_t t32 = tag >> 32;
  if (__builtin_expect(!__all(sa[1] == t32 && sb[1] == t32 && g0[1] == t32 && g1[1] == t32 && (MODE != 1 || g2[1] == t32)), 0)) {
    const uint32_t so = WS_STG + (rg * 128 + rb * 32 + r2) * 16, cv = r2 * 16 + (l2 >> 5) * 8;
    gran_fix(sa, ws, so, t32); gran_fix(sb, ws, so + 8, t32); gran_fix(g0, ws, CO0 + cv, t32); gran_fix(g1, ws, CO1 + cv, t32);
    if (MODE == 1) gran_fix(g2, ws, CO0 + 1024 + cv + ((l2 & 16) ? 560 * 16 : 0), t32);
  }
  const uint32_t um = sa[0], ur = sb[0], u0 = g0[0], u1 = g1[0], u2 = MODE == 1 ? g2[0] : 0u;
  mean = __builtin_bit_cast(float, um); rs = __builtin_bit_cast(float, ur);
  const float c0 = __builtin_bit_cast(float, u0), c1 = __builtin_bit_cast(float, u1), c2 = __builtin_bit_cast(float, u2);
  if (MODE == 0) {
    h_store(a0, a1, c0, c1, mean, rs, l2, rws, WS_A2 + ((rg * 8 + 2 * rb) * 68 + 2 * ci + nh) * 1024);
  } else {
    lds_wf((lptr)(RSV + w * 128 + r2 * 4), rs);
    const bf16x8 mf = split3(-mean, false);
    a0 = MFMA32(mf, split3(c0, true), a0); a1 = MFMA32(mf, split3(c1, true), a1);
    if (MODE == 1) a2 = MFMA32(mf, split3(c2, true), a2);
    const lptr ra = lp(RSV + w * 128 + (l2 >> 5) * 16);
#pragma unroll
    for (int g = 0; g < 4; ++g) {
      const f32x4 rv = lds_rf4(ra + 32 * g);
#pragma unroll
      for (int j = 4 * g; j < 4 * g + 4; ++j) {
        a0[j] *= rv[j & 3]; a1[j] *= rv[j & 3];
        if (MODE == 1) { a0[j] = phi(a0[j]); a1[j] = phi(a1[j]); const float v = a2[j] * rv[j & 3], p = phi(v); a2[j] = (l2 & 16) ? v : p; }
      }
    }
    const lptr e0 = lp(MISC + (((MODE - 1) * 12 + rb) * 2) * 1024 + l2 * 16);
    lds_w128(e0, pack8(a0, 0)); lds_w128(e0 + 1024, pack8(a0, 1));
    lds_w128(e0 + 8192, pack8(a1, 0)); lds_w128(e0 + 8192 + 1024, pack8(a1, 1));
    if (MODE == 1) { lds_w128(e0 + 16384, pack8(a2, 0)); lds_w128(e0 + 16384 + 1024, pack8(a2, 1)); }
  }
}
#define ORD 0
#define PUBP 1
#define KQ 0
constexpr uint32_t AOFF = KQ ? 69632 : 1024;
template <int NH>
DEV void q_chunk(u32x4 (&xf)[40], u32x4 (&A)[34], uint32_t as, float mean, float rs, int tid, int w, int ci, int rg, int ct,
                 uint32_t fb_row, rsrc_t rws, char* ws, u64 tag) {
  const int l = tid & 63, rb = w & 3;
  const uint32_t ta = (8 * (l >> 5) + ((l >> 2) & 3)) * 320 + ((l >> 4) & 1) * 32 + 8 * (l & 3) + NH * 128;
  const uint32_t ZS = MISC + 5120, X3 = MISC + 5632;
  const uint32_t l16 = l * 16;
  uint32_t cur = 2 * SLOT, prv = SLOT;
  f32x16 a0 = {}, a1 = {}; f32x4 pp; u32x2 f2;
  bf16x8 w0, w1, n0, n1;
  u32x2 g0, g1, fl; u32x4 z4; uint32_t z1;
  const uint32_t cv = (NH ? (l & 15) : (l & 31)) * 16 + (l >> 5) * 8, CO0 = WS_CSG + (80 * ci + 64 * NH) * 16;
  bar();
  lptr base = lp(cur + ta);
  w0 = trd2(base, base + 1280);
  if (NH == 0) w1 = trd2(base + 64, base + 64 + 1280);
#pragma unroll
  for (int s = 0; s < 5; ++s) {
#pragma unroll
    for (int t = 0; t < 8; ++t) {
      const int ks = 8 * s + t;
      SB0();
      if (t < 2 || (t == 2 && NH == 0))
        dma16_sc1(prv + (w + 8 * t) * 1024, l16, rws, WS_W2 + (ct * 340 + s * 20 + w + 8 * t) * 1024);
      if (NH == 0 && s == 2 && t == 0) publish_if0(w, rws, WS_FB + rg * 128 + ci * 8, (tag & ~0xffull) | 2);
      if (NH == 1 && s == 2 && t == 2) fl = aload64(rws, (l & 15) * 8, WS_FA + ci * 128);
      if (NH == 1 && s == 4 && t == 6) {
        fence_reg(z4); fence_reg(z1);
        const lptr zw = lp((l & 15) == 15 ? ZS + rb * 80 + (l >> 4) * 20 : ZS + 320);
#pragma unroll
        for (int j = 0; j < 5; ++j) {
          const uint32_t u = j < 4 ? z4[j & 3] : z1;
          float v = __builtin_bit_cast(float, u);
          v = dpp_add<0x111>(v); v = dpp_add<0x112>(v); v = dpp_add<0x114>(v); v = dpp_add<0x118>(v);
          lds_wf(zw + 4 * j, v);
        }
      }
      if (NH == 1 && s == 3 && t == 2) {
        wait_vm<2>(); acheck(fl, rws, (l & 15) * 8, WS_FA + ci * 128, (tag & ~0xffull) | 1);
        const uint32_t zv = (l & 15) * KVREC + (l >> 4) * 20, zo = WS_KVP + ci * 16 * KVREC + 6400 + rb * 80;
        z4 = aload128(rws, zv, zo); z1 = aload32(rws, zv, zo + 16);
      }
      if (s == 3 && t == 4) f2 = aload64(rws, (l & 7) * 8, fb_row);
      if (s == 4 && t >= 3) {
        if (t == 3) { wait_vm<3 - NH>(); acheck(f2, rws, (l & 7) * 8, fb_row, (tag & ~0xffull) | 2); }
        A[2 * t - 6] = aload128(rws, l16, as + (t - 3) * 4096); A[2 * t - 5] = aload128(rws, l16, as + (t - 3) * 4096 + AOFF);
      }
      if (s == 3 && t == 3) { g0 = aload64(rws, cv, CO0); if (NH == 0) g1 = aload64(rws, cv, CO0 + 512); }
      if (t < 7 || s < 4) {
        if (t == 7) {
          if (s == 1) open_stage<NH ? 4 : 6>(); else bar();
          prv = cur; cur = nxt(cur); base = lp(cur + ta);
        }
        const int tn = (t + 1) & 7;
        const lptr o = base + (tn & 3) * 5120 + (tn >> 2) * 160;
        n0 = trd2(o, o + 1280);
        if (NH == 0) n1 = trd2(o + 64, o + 64 + 1280);
      }
      SB0();
      pin<false>(ks, xf[ks]);
      a0 = MFMA32(w0, asbf(xf[ks]), a0);
      if (NH == 0) a1 = MFMA32(w1, asbf(xf[ks]), a1);
      w0 = n0; if (NH == 0) w1 = n1;
    }
  }
  SB0();
  STAMP(8);
  const int l2 = opq(tid) & 63, r = l2 & 31, hh = l2 >> 5;
  {
        fence_reg(g0); if (NH == 0) fence_reg(g1);
    const uint32_t u0 = g0[0], u1 = NH ? 0u : g1[0];
    const bf16x8 mf = split3(-mean, false);
    a0 = MFMA32(split3(__builtin_bit_cast(float, u0), true), mf, a0);
    if (NH == 0) a1 = MFMA32(split3(__builtin_bit_cast(float, u1), true), mf, a1);
  }
  const lptr zs = lp(ZS + 16 * hh), x3 = lp(X3 + (32 * rb + r) * 4);
  if (NH == 0) {
#pragma unroll
    for (int j = 0; j < 16; ++j) { a0[j] = phi(rs * a0[j]); a1[j] = phi(rs * a1[j]); }
    bar();
    float gs[8];
#pragma unroll
    for (int g = 0; g < 8; ++g) {
      const f32x4 z = lds_rf4(zs + 32 * g);
      float acc = 0.f;
#pragma unroll
      for (int j = 0; j < 4; ++j) acc = fmaf(g < 4 ? a0[4 * g + j] : a1[4 * (g - 4) + j], z[j], acc);
      gs[g] = acc;
    }
    float p0 = gs[0] + gs[1] + (hh ? 0.f : gs[2]);
    float p1 = gs[3] + gs[4] + (hh ? gs[2] : 0.f);
    float p2 = gs[5] + gs[6] + (hh ? 0.f : gs[7]);
    if (hh) lds_wf(x3, gs[7]);
    p0 += __shfl_xor(p0, 32); p1 += __shfl_xor(p1, 32); p2 += __shfl_xor(p2, 32);
    pp = f32x4{p0, p1, p2, gs[7]};
  } else {
#pragma unroll
    for (int j = 0; j < 8; ++j) a0[j] = phi(rs * a0[j]);
    bar();
    float p3 = 0.f;
#pragma unroll
    for (int g = 0; g < 2; ++g) {
      const f32x4 z = lds_rf4(zs + 256 + 32 * g);
#pragma unroll
      for (int j = 0; j < 4; ++j) p3 = fmaf(a0[4 * g + j], z[j], p3);
    }
    p3 += __shfl_xor(p3, 32);
    if (hh) lds_wf(x3 + 512, p3);
    pp = f32x4{p3, p3, p3, p3};
  }
  const uint32_t pv = (r >> 4) * (68 * 1024) + (hh * 16 + (r & 15)) * 16;
  const uint32_t qb = WS_A2 + ((rg * 8 + 2 * rb) * 68 + 48) * 1024 + ci * 5 * 512;
  bar();
  const float r3 = __builtin_amdgcn_rcpf(pp[3] + lds_rf(x3 + (NH ? 0 : 512)) + 1e-6f);
  if (NH == 0) {
    const float r0 = __builtin_amdgcn_rcpf(pp[0] + 1e-6f), r1 = __builtin_amdgcn_rcpf(pp[1] + 1e-6f), r2 = __builtin_amdgcn_rcpf(pp[2] + 1e-6f);
    const float sc[8] = {r0, r0, hh ? r1 : r0, r1, r1, r2, r2, hh ? r3 : r2};
#pragma unroll
    for (int j = 0; j < 16; ++j) { a0[j] = a0[j] * sc[j >> 2]; a1[j] = a1[j] * sc[4 + (j >> 2)]; }
    st128_sc1(pack8(a0, 0), rws, pv, qb); st128_sc1(pack8(a0, 1), rws, pv, qb + 512);
    st128_sc1(pack8(a1, 0), rws, pv, qb + 1024); st128_sc1(pack8(a1, 1), rws, pv, qb + 1536);
  } else {
#pragma unroll
    for (int j = 0; j < 8; ++j) a0[j] = a0[j] * r3;
    st128_sc1(pack8(a0, 0), rws, pv, qb + 2048);
  }
}

__global__ __launch_bounds__(512, 2) void fused_kernel(const void* __restrict__ gx, const void* __restrict__ gwqkv,
                                                       const void* __restrict__ gwo, const void* __restrict__ gwg,
                                                       const void* __restrict__ gwu, const void* __restrict__ gwd,
                                                       void* __restrict__ gout, char* __restrict__ ws) {
  const int tid = threadIdx.x;
  const int w = __builtin_amdgcn_readfirstlane(tid >> 6);
  const int rb = w & 3, nh = w >> 2;
  const int b = blockIdx.x;
  const int xcd = b & 7, idx = b >> 3;
  const int rg = (xcd >> 2) * 8 + (idx >> 2);
  const bool iskv = (idx & 2) != 0;
  const int ci = 2 * (xcd & 3) + (idx & 1);
  const int tt = (xcd >> 1) * 8 + (idx >> 2), ct = (xcd & 1) * 4 + (idx & 3);

  const rsrc_t rx = mk_rsrc(gx, 2048 * 1280), rqkv = mk_rsrc(gwqkv, 640 * 3840);
  const rsrc_t rg_ = mk_rsrc(gwg, 640 * 3072), ru_ = mk_rsrc(gwu, 640 * 3072);
  const rsrc_t rws = mk_rsrc(ws, WS_USED);
  STAMP(0);
  {
    u64 pc; asm volatile("s_getpc_b64 %0" : "=s"(pc));
    const uint32_t l = tid & 63;
    asm volatile("s_mov_b32 m0, %0\n\ts_nop 0\n\tbuffer_load_dword %1, %2, 0 offen lds"
                 :: "s"(6 * SLOT + 16384), "v"(l < 2 ? (idx * 16 + w * 2 + l) * 128 : OOB), "s"(mk_rsrc((const void*)pc, 65536)) : "memory");
  }

  u64 tag;
  u32x4 xf[40];
  float mean, rs;
  const uint32_t mode = ((idx >> 1) & 1) * (1 + nh);
  if (mode == 0) {
    first_chunk<0>(xf, mean, rs, tag, opq(tid), w, b, rg, ci, rx, rqkv, rg_, ru_, gwd, rws, ws);
  }
  if (opqs(mode) == 1) first_chunk<1>(xf, mean, rs, tag, opq(tid), w, b, rg, ci, rx, rqkv, rg_, ru_, gwd, rws, ws);
  if (opqs(mode) == 2) first_chunk<2>(xf, mean, rs, tag, opq(tid), w, b, rg, ci, rx, rqkv, rg_, ru_, gwd, rws, ws);
  if (iskv) {
    const int t2 = opq(tid), l = t2 & 63, hh = l >> 5, r = l & 31;
    bar();
    {
      const uint32_t guvo = (8 * w + (l >> 3)) * 3072 + (((l & 7) ^ (((l >> 4) & 1) << 2)) << 4), so = 4 * 64 * 3072 + (16 + ci) * 128;
      dma16(2 * SLOT + w * 1024, guvo, rg_, so); dma16(2 * SLOT + 8192 + w * 1024, guvo, ru_, so);
    }
    const int kb = w < 5 ? (w >> 1) : (w - 5), vb = 2 + ((w + 1) >> 1);
    const lptr ka = lp(MISC + kb * 8192 + l * 16), va = lp(MISC + vb * 8192 + l * 16);
    f32x16 d = {};
    const uint32_t ck = 3 * SLOT;
    if (w < 5) {
#pragma unroll
      for (int j = 0; j < 8; ++j) d = MFMA32(asbf(lds_r128(ka + j * 1024)), asbf(lds_r128(va + j * 1024)), d);
      const int ve = 32 * vb + r - 80;
      const int hv = (ve * 205) >> 12;
      const int e = ve - 20 * hv;
      const int lo = 20 * hv - 32 * kb - 4 * hh;
      const uint32_t ca = ck + (hv * 400 + e - lo * 20) * 4;
#pragma unroll
      for (int j = 0; j < 16; ++j) {
        const int rowb = (j & 3) + 8 * (j >> 2);
        if (ve >= 0 && (unsigned)(rowb - lo) < 20u) lds_wf((lptr)(ca + rowb * 80), d[j]);
      }
    } else {
      const u32x4 ones = {0x3f803f80u, 0x3f803f80u, 0x3f803f80u, 0x3f803f80u};
#pragma unroll
      for (int j = 0; j < 8; ++j) d = MFMA32(asbf(ones), asbf(lds_r128(ka + j * 1024)), d);
      if (hh == 0 && 32 * kb + r < 80) lds_wf((lptr)(ck + 6400 + (32 * kb + r) * 4), d[0]);
    }
    bar();
    st128_sc1(lds_r128((lptr)(ck + t2 * 16)), rws, t2 < 420 ? t2 * 16 : OOB, WS_KVP + (ci * 16 + rg) * KVREC);
  }
  STAMP(3);

  const uint32_t fb_row = WS_FB + (tt >> 1) * 128;
  u32x2 pl; u32x4 wo4, k0, k1, k2, k3;
  {
    const int l = opq(tid) & 63;
    const uint32_t ga = (8 * (l >> 5) + ((l >> 2) & 3)) * 128 + 64 * (nh ^ ((l >> 3) & 1)) + 32 * ((l >> 4) & 1) + 8 * (l & 3);
    const uint32_t guvo = (8 * w + (l >> 3)) * 3072 + (((l & 7) ^ (((l >> 4) & 1) << 2)) << 4);
    const int c = iskv ? 16 + ci : 8 + ci;
    uint32_t nv0, nv1, nv2, nso, nstr; rsrc_t nr0;
    if (!iskv) { nv0 = kvq_voff(w, l, ci, true); nv1 = kvq_voff(w + 8, l, ci, true); nv2 = kvq_voff((w & 3) + 16, l, ci, true);
                 nr0 = rqkv; nso = 0; nstr = 128 * 3840; }
    else { nv0 = l * 16; nv1 = l * 16 + 8192; nv2 = l * 16 + 16384; nr0 = rws; nso = WS_W2 + (ct * 340 + w) * 1024; nstr = 20480; }
    nso = __builtin_amdgcn_readfirstlane(nso); nstr = __builtin_amdgcn_readfirstlane(nstr);
    const uint32_t third = nh ^ 1, wk = w * 1024;
    const uint32_t gso = opqs(c * 128);
    f32x16 ag = {}, au = {};
    bf16x8 w0, w1;
    u32x2 g0, g1, fl;
    const u64 tg1 = (tag & ~0xffull) | 1;
    const uint32_t isq = (~idx >> 1) & 1, fao = iskv ? WS_FA + ci * 128 : WS_FT;
    bar();
    lptr base = lp(4 * SLOT + ga);
    w0 = trd2(base, base + 512); w1 = trd2(base + 8192, base + 8192 + 512);
#pragma unroll
    for (int s = 0; s < 10; ++s) {
#pragma unroll
      for (int t = 0; t < 4; ++t) {
        const int ks = 4 * s + t;
        const uint32_t prv = ((3 + s) % 6) * SLOT + wk;
        lptr o = base + (t + 1) * 2048;
        SB0();
        pin<true>(ks, xf[ks]);
        ag = MFMA32(w0, asbf(xf[ks]), ag);
        SB0();
        if (t == 3 && s < 9) {
          if (s == 0) bar(); else if (s == 1) open_stage<9>(); else if (s == 2) open_stage<10>();
          else if (s == 3) open_stage2<10, 9>(isq); else if (s == 8) open_stage<8>(); else open_stage<8>();
          o = base = lp(((5 + s) % 6) * SLOT + ga);
        }
        if (ks < 39) w0 = trd2(o, o + 512);
        if (s == 0 && t == 0) dma16_if(isq, 2 * SLOT + wk, guvo, rg_, 4 * 64 * 3072 + gso);
        if (s < 5) {
          if (t == 1) dma16(prv, guvo, rg_, (s + 5) * 64 * 3072 + gso);
          if (t == 2) dma16(prv + 8192, guvo, ru_, (s + 5) * 64 * 3072 + gso);
        } else {
          if (t == 0) dma16_sc1(prv, nv0, nr0, nso);
          if (t == 1) dma16_sc1(prv + 8192, nv1, nr0, nso);
          if (t == 2) { dma16_if(third, prv + 16384, nv2, nr0, nso); nso += nstr; }
        }
        SB0();
        au = MFMA32(w1, asbf(xf[ks]), au);
        SB0();
        if (ks < 39) w1 = trd2(o + 8192, o + 8192 + 512);
        if (s == 0 && t == 0) dma16_if(isq, 2 * SLOT + 8192 + wk, guvo, ru_, 4 * 64 * 3072 + gso);
        if (s == 2 && t == 0) fl = aload64(rws, (l & 31) * 8, WS_FT + w * 256);
        if (s == 4 && t == 3) publish_if0((~idx & 2) | w, rws, WS_FA + (ci * 16 + rg) * 8, tg1);
        if (s == 4 && t == 2) { wait_vm<6>(); acheck(fl, rws, (l & 31) * 8, WS_FT + w * 256, tg1); }
        if (s == 5 && t == 2) publish_if0(b | w, rws, WS_E, tag);
        if (s == 7 && t == 2) {
          const uint32_t cv = (l & 31) * 16 + (l >> 5) * 8, co = WS_CSG + (1920 + 64 * c + 32 * nh) * 16;
          g0 = aload64(rws, cv, co); g1 = aload64(rws, cv, co + 1536 * 16);
        }
        if (s == 7 && t == 3) fl = aload64(rws, (l & 15) * 8, fao);
        if (s == 9 && t == 2) {
          wait_vm2<6, 4>(nh); acheck(fl, rws, (l & 15) * 8, fao, tg1);
          const int t3 = opq(tid), row = (t3 * 3277) >> 16, rq = (t3 * 656) >> 16;
          const bool on = t3 < 400;
          const uint32_t vo = on ? rq * 4 * KVREC + (t3 - 100 * rq) * 16 : OOB, so = WS_KVP + ci * 16 * KVREC + (rg & 3) * 1600;
          asm volatile("s_cmp_lg_u32 %15, 0\n\ts_cbranch_scc1 1f\n\tbuffer_load_dwordx4 %0, %6, %9, %11 offen sc1\n\t"
                       "buffer_load_dwordx4 %1, %6, %9, %12 offen sc1\n\tbuffer_load_dwordx4 %2, %6, %9, %16 offen sc1\n\t"
                       "buffer_load_dwordx4 %3, %6, %9, %17 offen sc1\n\tbuffer_load_dwordx4 %4, %7, %10, %13 offen\n\t"
                       "buffer_load_dwordx2 %5, %8, %9, %14 offen sc1\n1:"
                       : "=&v"(k0), "=&v"(k1), "=&v"(k2), "=&v"(k3), "=&v"(wo4), "=&v"(pl)
                       : "v"(vo), "v"(on ? row * 1280 + (t3 - 20 * row) * 16 : OOB), "v"((l & 7) * 8), "s"(rws), "s"(mk_rsrc(gwo, 640 * 1280)),
                         "s"(so), "s"(so + KVREC), "s"((4 * ci + (rg & 3)) * 20 * 1280 + (rg >> 2) * 320), "s"(fb_row), "s"(isq),
                         "s"(so + 2 * KVREC), "s"(so + 3 * KVREC) : "memory", "scc");
        }
      }
    }
    SB0();
    STAMP(6);
    const int l2 = opq(tid) & 63;
    fence_reg(g0); fence_reg(g1);
    const uint32_t u0 = g0[0], u1 = g1[0];
    h_store(ag, au, __builtin_bit_cast(float, u0), __builtin_bit_cast(float, u1), mean, rs, l2, rws, WS_A2 + ((rg * 8 + 2 * rb) * 68 + 2 * c + nh) * 1024);
    STAMP(7);
  }
  uint32_t cur = 2 * SLOT, prv = SLOT;

  u32x4 A[34];
  const uint32_t as = WS_A2 + (KQ ? (tt * 4 + 2 * (w & 1)) * 68 + (w >> 1) : (tt * 4 + rb) * 68 + 2 * nh) * 1024;
  const uint32_t role = iskv ? 2 : nh;
  const uint32_t WO = MISC, RED = MISC + 6400;
  if (role == 1) q_chunk<1>(xf, A, as, mean, rs, opq(tid), w, ci, rg, ct, fb_row, rws, ws, tag);
  else if (role == 0) q_chunk<0>(xf, A, as, mean, rs, opq(tid), w, ci, rg, ct, fb_row, rws, ws, tag);
  else {
    const int t4 = opq(tid), l = t4 & 63;
    wait_vm<2>();
    fence_reg(wo4); fence_reg(k0); fence_reg(k1); fence_reg(k2); fence_reg(k3);
    if (t4 < 400) {
      lds_w128((lptr)(WO + t4 * 16), wo4);
      f32x4 sum = (__builtin_bit_cast(f32x4, k0) + __builtin_bit_cast(f32x4, k1)) + (__builtin_bit_cast(f32x4, k2) + __builtin_bit_cast(f32x4, k3));
      lds_w128((lptr)(RED + t4 * 16), __builtin_bit_cast(u32x4, sum));
    }
    acheck(pl, rws, (t4 & 7) * 8, fb_row, (tag & ~0xffull) | 2);
#pragma unroll
    for (int p = 0; p < 5; ++p) { A[2 * p] = aload128(rws, l * 16, as + p * 4096); A[2 * p + 1] = aload128(rws, l * 16, as + p * 4096 + AOFF); }
    bar();
    STAMP(8);
    const int hl = rg & 3, qd = rg >> 2, head = 4 * ci + hl;
    const int q4 = (l >> 2) & 3, p4 = l & 3;
    {
      const int i16 = l & 15, g = l >> 4;
      u32x4 af[2];
#pragma unroll
      for (int db = 0; db < 2; ++db) {
        const int d = min(16 * db + i16, 19);
        f32x4 lo = {}, hi = {};
        const lptr al = lp(RED + d * 80 + min(g, 2) * 32), ah = lp(RED + d * 80 + min(g, 2) * 32 + (g < 2 ? 16 : 0));
#pragma unroll
        for (int j = 0; j < 4; ++j) {
          lo += lds_rf4(al + j * 1600);
          hi += lds_rf4(ah + j * 1600);
        }
        af[db][0] = g < 3 ? pk2(lo[0], lo[1]) : 0u; af[db][1] = g < 3 ? pk2(lo[2], lo[3]) : 0u;
        af[db][2] = g < 2 ? pk2(hi[0], hi[1]) : 0u; af[db][3] = g < 2 ? pk2(hi[2], hi[3]) : 0u;
      }
      const uint32_t b0 = WO + min(8 * g + q4, 16 + q4) * 320 + 8 * p4, b1 = WO + min(8 * g + 4 + q4, 16 + q4) * 320 + 8 * p4;
      for (int n = w; n < 10; n += 8) {
        const bf16x8 bf = trd2((lptr)(b0 + n * 32), (lptr)(b1 + n * 32));
        const f32x4 z4 = {};
        const f32x4 m0 = MFMA16(asbf(af[0]), bf, z4), m1 = MFMA16(asbf(af[1]), bf, z4);
        const int cb = 10 * qd + n, c5 = cb / 5, n5 = cb - 5 * c5;
        const uint32_t fb = WS_W2 + (c5 * 340 + 240 + n5) * 1024 + i16 * 16;
        const int G0 = 5 * head + g, G1 = 5 * head + 4;
        const uint32_t o0 = fb + (G0 >> 3) * 5120 + (((G0 >> 2) & 1) * 2 + (G0 & 1)) * 256 + ((G0 >> 1) & 1) * 8;
        const uint32_t o1 = fb + (G1 >> 3) * 5120 + (((G1 >> 2) & 1) * 2 + (G1 & 1)) * 256 + ((G1 >> 1) & 1) * 8;
        u32x2 v0 = {pk2(m0[0], m0[1]), pk2(m0[2], m0[3])}, v1 = {pk2(m1[0], m1[1]), pk2(m1[2], m1[3])};
        __builtin_amdgcn_raw_buffer_store_b64(v0, rws, o0, 0, 16);
        if (g == 0) __builtin_amdgcn_raw_buffer_store_b64(v1, rws, o1, 0, 16);
      }
    }
  }
  STAMP(9);
  const int t5 = opq(tid), l = t5 & 63;
  const int pr0 = (t5 * 6554) >> 16;
  const int pr1 = ((t5 + 512) * 6554) >> 16;
  const uint32_t xo0 = pr0 * 1280 + (t5 - 10 * pr0) * 16, xo1 = t5 < 128 ? pr1 * 1280 + (t5 + 512 - 10 * pr1) * 16 : OOB;
  const uint32_t xs = tt * 64 * 1280 + ct * 160;
  const uint32_t l16 = l * 16;
  if (!iskv) { cur = SLOT; prv = 0; }
  const uint32_t pub_off = iskv ? WS_FM + ((rg >> 2) * 32 + 4 * ci + (rg & 3)) * 8 : WS_FB + rg * 128 + ci * 8;

  {
    const int tid = t5;
    u32x4 xr0 = aload128p(rx, xo0, xs), xr1 = aload128p(rx, xo1, xs);
    const uint32_t po2 = fb_row + 64 + (l & 7) * 8;
    const uint32_t po3 = l < 32 ? WS_FM + (ct >> 1) * 256 + l * 8 : fb_row + (l & 7) * 8;
    const u64 tg3 = (tag & ~0xffull) | 3;
    f32x4 acc[10] = {};
    u32x4 B[2][10];
#define XS2(i)
#pragma unroll
    for (int p = 0; p < 17; ++p) {
      const int lo = p < 5 ? 0 : p - 4, hi = p - 1 < 11 ? p - 1 : 11;
      const int nop = hi >= lo ? hi - lo + 1 : 0, ex = p < PUBP ? 2 * (4 - p) + 3 : p == PUBP ? 2 : 0;
      if (p == 4) { wait_vm<8>(); XS2(17); acheck(pl, rws, po2, 0, tg3); }
      if (p == 7) { wait_vm<10>(); XS2(18); acheck(pl, rws, po3, 0, tg3); }
      switch (nop * 16 + ex) {
#define OPEN_CASE(NOP, EX) case NOP * 16 + EX: if (p < 12) open_stage<NOP * 5 + EX>(); else open_nl<NOP * 5 + EX>(); break;
        OPEN_CASE(0, 11) OPEN_CASE(1, 9) OPEN_CASE(2, 2) OPEN_CASE(1, 2) OPEN_CASE(4, 0)
        OPEN_CASE(3, 0) OPEN_CASE(2, 0) OPEN_CASE(1, 0) OPEN_CASE(0, 0)
        default: open_stage<0>();
      }
      fence_reg(A[2 * p]); fence_reg(A[2 * p + 1]);
      if (p == 0) publish_if0((~idx & 2) | w, rws, WS_FB + rg * 128 + (8 + ci) * 8, tg3);
      if (p == PUBP) publish_if0(w, rws, pub_off, tg3);
      if (p == 2 || p == 5) pl = aload64(rws, p == 2 ? po2 : po3, 0);
      if (p == 4) { A[16] = aload128(rws, l16, as + 8 * 4096); A[17] = aload128(rws, l16, as + 8 * 4096 + AOFF); }
      const lptr rbs = lp(cur + (KQ ? (w >> 1) * 5120 : nh * 10240) + l16);
#define PM(J) if (p > 0) { PM1(2 * J); PM1(2 * J + 1); } SB0()
#define PM1(J) acc[KQ ? (J) : (J) % 5] = MFMA16(asbf(B[q ^ 1][KQ ? (J) / 2 : (J)]), asbf(A[2 * p - 2 + (KQ ? (J) & 1 : (J) / 5)]), acc[KQ ? (J) : (J) % 5])
      const int q = p & 1;
      const bool ld = p + 5 < 17;
      const uint32_t so = WS_W2 + (ct * 340 + (p + 5) * 20) * 1024;
      if (ld) { dma16_sc1(prv + w * 1024, l16, rws, so + w * 1024); if (ORD) dma16_sc1(prv + (w + 8) * 1024, l16, rws, so + (w + 8) * 1024); }
      SB0();
#pragma unroll
      for (int j = 0; j < (KQ ? 5 : 10); ++j) B[q][j] = lds_r128(rbs + j * 1024);
      SB0();
      PM(0);
      if (ld && !ORD) dma16_sc1(prv + (w + 8) * 1024, l16, rws, so + (w + 8) * 1024);
      SB0(); PM(1);
      if (ld) dma8_sc1(prv + 16384 + w * 512, l16, rws, so + 16384 + w * 512);
      SB0(); PM(2); PM(3);
      if (ld && p != 3) A[2 * p + 10] = aload128(rws, l16, as + (p + 5) * 4096);
      SB0(); PM(4);
      if (ld && p != 3) A[2 * p + 11] = aload128(rws, l16, as + (p + 5) * 4096 + AOFF);
      SB0();
      prv = cur; cur = nxt(cur);
    }
#pragma unroll
    for (int j = 0; j < 10; ++j) { const int p = 17, q = 1; PM1(j); }
    STAMP(10);
    const uint32_t fb = iskv ? SLOT : 0;
    if (KQ) bar();
    const lptr ow = lp(fb + (w * (KQ ? 32 : 16) + (l & 15)) * 336 + (l >> 4) * 16);
#pragma unroll
    for (int j = 0; j < (KQ ? 10 : 5); ++j) lds_w128(ow + (KQ ? (j >> 1) * 64 + (j & 1) * 16 * 336 : j * 64), __builtin_bit_cast(u32x4, acc[j]));
    bar();
    const rsrc_t ro = mk_rsrc(gout, 2048 * 1280);
#pragma unroll
    for (int h = 0; h < 2; ++h) {
      if (h == 0 || tid < 128) {
        u32x4 xr = h ? xr1 : xr0;
        fence_reg(xr);
        const int pr = h ? pr1 : pr0, pc = tid + 512 * h - 10 * pr;
        const lptr a = lp(fb + pr * 336 + pc * 32);
        f32x4 u0 = (lds_rf4(a) + lds_rf4(a + 64 * 336));
        f32x4 u1 = (lds_rf4(a + 16) + lds_rf4(a + 16 + 64 * 336));
        if (KQ) { u0 += lds_rf4(a + 128 * 336) + lds_rf4(a + 192 * 336); u1 += lds_rf4(a + 16 + 128 * 336) + lds_rf4(a + 16 + 192 * 336); }
        u32x4 o;
        o[0] = pk2(u0[0] + blo(xr[0]), u0[1] + bhi(xr[0])); o[1] = pk2(u0[2] + blo(xr[1]), u0[3] + bhi(xr[1]));
        o[2] = pk2(u1[0] + blo(xr[2]), u1[1] + bhi(xr[2])); o[3] = pk2(u1[2] + blo(xr[3]), u1[3] + bhi(xr[3]));
        asm volatile("buffer_store_dwordx4 %0, %1, %2, %3 offen\n\ts_nop 1" :: "v"(o), "v"(h ? xo1 : xo0), "s"(ro), "s"(xs) : "memory");
      }
    }
    STAMP(11);
  }
}

extern "C" void kernel_launch(void* const* d_in, const int* in_sizes, int n_in, void* d_out, int out_size, void* d_ws,
                              size_t ws_size, hipStream_t stream) {
  (void)in_sizes; (void)n_in; (void)out_size; (void)ws_size;
  (void)hipFuncSetAttribute((const void*)fused_kernel, hipFuncAttributeMaxDynamicSharedMemorySize, LDS_BYTES);
  fused_kernel<<<256, 512, LDS_BYTES, stream>>>(d_in[0], d_in[1], d_in[2], d_in[4], d_in[5], d_in[6], d_out, (char*)d_ws);
}
```

```cpp
#include <hip/hip_runtime.h>
#include <stdint.h>

typedef __attribute__((ext_vector_type(8))) __bf16 bf16x8;
typedef __attribute__((ext_vector_type(4))) __bf16 bf16x4;
typedef __attribute__((ext_vector_type(2))) __bf16 bf16x2;
typedef __attribute__((ext_vector_type(16))) float f32x16;
typedef __attribute__((ext_vector_type(4))) float f32x4;
typedef __attribute__((ext_vector_type(2))) float f32x2;
typedef __attribute__((ext_vector_type(4))) unsigned u32x4;
typedef __attribute__((ext_vector_type(2))) unsigned u32x2;
typedef __amdgpu_buffer_rsrc_t rsrc_t;
typedef unsigned long long u64;
#define AS3 __attribute__((address_space(3)))
#define AS1 __attribute__((address_space(1)))
#define DEV __device__ __forceinline__
#define SB0() __builtin_amdgcn_sched_barrier(0)

constexpr uint32_t SLOT = 20480, RING = 122880, MISC = RING, LDS_BYTES = 163840;
constexpr uint32_t WS_E = 0, WS_FT = 4096, WS_FA = 8192, WS_FB = 12288, WS_FM = 16384;
constexpr uint32_t WS_KVP = 32768, KVREC = 6912;
constexpr uint32_t WS_W2 = 1u << 20, WS_A2 = 4u << 20;
constexpr uint32_t WS_USED = WS_A2 + 128u * 68u * 1024u;
constexpr uint32_t SPIN_CAP = 1u << 16;

DEV rsrc_t mk_rsrc(const void* p, uint32_t bytes) {
  return __builtin_amdgcn_make_buffer_rsrc((void*)p, 0, bytes, 0x00020000);
}
DEV void dma16(uint32_t m0v, uint32_t voff, rsrc_t srd, uint32_t soff) {
  asm volatile("s_mov_b32 m0, %0\n\ts_nop 0\n\tbuffer_load_dwordx4 %1, %2, %3 offen lds"
               :: "s"(m0v), "v"(voff), "s"(srd), "s"(soff) : "memory");
}
DEV void dma16_sc1(uint32_t m0v, uint32_t voff, rsrc_t srd, uint32_t soff) {
  asm volatile("s_mov_b32 m0, %0\n\ts_nop 0\n\tbuffer_load_dwordx4 %1, %2, %3 offen sc1 lds"
               :: "s"(m0v), "v"(voff), "s"(srd), "s"(soff) : "memory");
}
DEV void dma16_if(uint32_t cond, uint32_t m0v, uint32_t voff, rsrc_t srd, uint32_t soff) {
  asm volatile("s_cmp_eq_u32 %4, 0\n\ts_cbranch_scc1 1f\n\ts_mov_b32 m0, %0\n\ts_nop 0\n\t"
               "buffer_load_dwordx4 %1, %2, %3 offen sc1 lds\n1:"
               :: "s"(m0v), "v"(voff), "s"(srd), "s"(soff), "s"(cond) : "memory", "scc");
}
DEV void dma8_sc1(uint32_t m0v, uint32_t voff, rsrc_t srd, uint32_t soff) {
  asm volatile("s_mov_b32 m0, %0\n\ts_mov_b32 exec_hi, 0\n\tbuffer_load_dwordx4 %1, %2, %3 offen sc1 lds\n\ts_mov_b32 exec_hi, -1"
               :: "s"(m0v), "v"(voff), "s"(srd), "s"(soff) : "memory");
}
template <int N> DEV void open_nl() { asm volatile("s_waitcnt vmcnt(%0)\n\ts_barrier" :: "n"(N) : "memory"); }
template <int N> DEV void wait_vm() { asm volatile("s_waitcnt vmcnt(%0)" :: "n"(N) : "memory"); }
DEV void bar() { asm volatile("s_waitcnt lgkmcnt(0)\n\ts_barrier" ::: "memory"); }
template <int N> DEV void open_stage() {
  asm volatile("s_waitcnt vmcnt(%0) lgkmcnt(0)\n\ts_barrier" :: "n"(N) : "memory");
}
template <int N0, int N1> DEV void open_stage2(uint32_t sel) {
  asm volatile("s_cmp_eq_u32 %2, 0\n\ts_cbranch_scc1 1f\n\ts_waitcnt vmcnt(%1)\n1:\n\ts_waitcnt vmcnt(%0) lgkmcnt(0)\n\ts_barrier"
               :: "n"(N0), "n"(N1), "s"(sel) : "memory", "scc");
}
template <int N0, int N1> DEV void wait_vm2(uint32_t sel) {
  asm volatile("s_cmp_eq_u32 %2, 0\n\ts_cbranch_scc1 1f\n\ts_waitcnt vmcnt(%1)\n1:\n\ts_waitcnt vmcnt(%0)" :: "n"(N0), "n"(N1), "s"(sel) : "memory", "scc");
}
DEV void publish_if0(uint32_t cond, rsrc_t r, uint32_t soff, u64 val) {
  u64 t;
  asm volatile("s_cmp_lg_u32 %4, 0\n\ts_cbranch_scc1 1f\n\tv_mov_b64 %0, %1\n\ts_mov_b64 exec, 1\n\t"
               "buffer_store_dwordx2 %0, off, %2, %3 sc1\n\ts_mov_b64 exec, -1\n1:"
               : "=&v"(t) : "s"(val), "s"(r), "s"(soff), "s"(cond) : "memory", "scc");
}
typedef AS3 char* lptr;
DEV lptr lp(uint32_t a) { asm volatile("" : "+v"(a)); return (lptr)a; }
DEV bf16x4 trd(lptr a) { return __builtin_amdgcn_ds_read_tr16_b64_v4bf16((AS3 bf16x4*)a); }
DEV bf16x8 trd2(lptr a0, lptr a1) {
  bf16x4 lo = trd(a0), hi = trd(a1);
  return __builtin_shufflevector(lo, hi, 0, 1, 2, 3, 4, 5, 6, 7);
}
DEV u32x4 lds_r128(lptr a) { return *(AS3 u32x4*)a; }
DEV f32x4 lds_rf4(lptr a) { return *(AS3 f32x4*)a; }
DEV void lds_w128(lptr a, u32x4 v) { *(AS3 u32x4*)a = v; }
DEV float lds_rf(lptr a) { return *(AS3 float*)a; }
DEV void lds_wf(lptr a, float v) { *(AS3 float*)a = v; }
DEV int opq(int v) { asm volatile("" : "+v"(v)); return v; }
DEV uint32_t opqs(uint32_t v) { asm volatile("" : "+s"(v)); return v; }
DEV uint32_t pk2(float a, float b) {
  f32x2 v = {a, b};
  return __builtin_bit_cast(uint32_t, __builtin_convertvector(v, bf16x2));
}
DEV float blo(uint32_t u) { return __builtin_bit_cast(float, u << 16); }
DEV float bhi(uint32_t u) { return __builtin_bit_cast(float, u & 0xffff0000u); }
DEV f32x2 phi2(f32x2 a, f32x2 r, f32x2 r2) {
  const f32x2 eps = {1e-6f, 1e-6f}, t = __builtin_elementwise_fma(a, r, eps), u = a * r2;
  const f32x2 e = {__builtin_amdgcn_fmed3f(__builtin_amdgcn_exp2f(u[0]), 0.f, 1.f), __builtin_amdgcn_fmed3f(__builtin_amdgcn_exp2f(u[1]), 0.f, 1.f)};
  const f32x2 m = {fmaxf(t[0], 1e-6f), fmaxf(t[1], 1e-6f)};
  return m + e;
}
#define PHI2(A, J, R, R2) do { const f32x2 o_ = phi2(f32x2{A[J], A[(J) + 1]}, R, R2); A[J] = o_[0]; A[(J) + 1] = o_[1]; } while (0)
constexpr float LOG2E = 1.4426950408889634f;
#define MFMA32(a, b, c) __builtin_amdgcn_mfma_f32_32x32x16_bf16(a, b, c, 0, 0, 0)
#define MFMA16(a, b, c) __builtin_amdgcn_mfma_f32_16x16x32_bf16(a, b, c, 0, 0, 0)
DEV bf16x8 asbf(u32x4 v) { return __builtin_bit_cast(bf16x8, v); }
DEV u32x4 pack8(const f32x16& a, int s) {
  u32x4 r;
  r[0] = pk2(a[8 * s + 0], a[8 * s + 1]); r[1] = pk2(a[8 * s + 2], a[8 * s + 3]);
  r[2] = pk2(a[8 * s + 4], a[8 * s + 5]); r[3] = pk2(a[8 * s + 6], a[8 * s + 7]);
  return r;
}
DEV void st128_sc1(u32x4 v, rsrc_t r, uint32_t voff, uint32_t soff) {
  asm volatile("buffer_store_dwordx4 %0, %1, %2, %3 offen sc1\n\ts_nop 1" :: "v"(v), "v"(voff), "s"(r), "s"(soff) : "memory");
}
DEV void flag_store(char* ws, uint32_t off, u64 v) {
  __hip_atomic_store((AS1 u64*)(ws + off), v, __ATOMIC_RELAXED, __HIP_MEMORY_SCOPE_AGENT);
}
DEV u64 flag_load(char* ws, uint32_t off) {
  return __hip_atomic_load((AS1 u64*)(ws + off), __ATOMIC_RELAXED, __HIP_MEMORY_SCOPE_AGENT);
}
DEV bool flag_ok(u64 v, u64 tag, uint32_t lvl) { return ((v ^ tag) >> 8) == 0 && (uint32_t)(v & 0xff) >= lvl; }
DEV u32x2 aload64(rsrc_t r, uint32_t voff, uint32_t soff) {
  u32x2 v; asm volatile("buffer_load_dwordx2 %0, %1, %2, %3 offen sc1" : "=v"(v) : "v"(voff), "s"(r), "s"(soff) : "memory"); return v;
}
DEV uint32_t aload32(rsrc_t r, uint32_t voff, uint32_t soff) {
  uint32_t v; asm volatile("buffer_load_dword %0, %1, %2, %3 offen sc1" : "=v"(v) : "v"(voff), "s"(r), "s"(soff) : "memory"); return v;
}
DEV u32x4 aload128(rsrc_t r, uint32_t voff, uint32_t soff) {
  u32x4 v; asm volatile("buffer_load_dwordx4 %0, %1, %2, %3 offen sc1" : "=v"(v) : "v"(voff), "s"(r), "s"(soff) : "memory"); return v;
}
DEV u32x4 aload128p(rsrc_t r, uint32_t voff, uint32_t soff) {
  u32x4 v; asm volatile("buffer_load_dwordx4 %0, %1, %2, %3 offen" : "=v"(v) : "v"(voff), "s"(r), "s"(soff) : "memory"); return v;
}
template <typename T> DEV void fence_reg(T& v) { asm volatile("" : "+v"(v) :: "memory"); }
DEV u64 u64of(u32x2 v) { return ((u64)v[1] << 32) | v[0]; }
#define PINX(I, A, D) case I: if (RW) asm volatile("" : "+{v[" #A ":" #D "]}"(f)); else asm volatile("" :: "{v[" #A ":" #D "]}"(f)); break;
template <bool RW> DEV void pin(int i, u32x4& f) {
  switch (i) {
    PINX(0, 96, 99) PINX(1, 100, 103) PINX(2, 104, 107) PINX(3, 108, 111) PINX(4, 112, 115)
    PINX(5, 116, 119) PINX(6, 120, 123) PINX(7, 124, 127) PINX(8, 128, 131) PINX(9, 132, 135)
    PINX(10, 136, 139) PINX(11, 140, 143) PINX(12, 144, 147) PINX(13, 148, 151) PINX(14, 152, 155)
    PINX(15, 156, 159) PINX(16, 160, 163) PINX(17, 164, 167) PINX(18, 168, 171) PINX(19, 172, 175)
    PINX(20, 176, 179) PINX(21, 180, 183) PINX(22, 184, 187) PINX(23, 188, 191) PINX(24, 192, 195)
    PINX(25, 196, 199) PINX(26, 200, 203) PINX(27, 204, 207) PINX(28, 208, 211) PINX(29, 212, 215)
    PINX(30, 216, 219) PINX(31, 220, 223) PINX(32, 224, 227) PINX(33, 228, 231) PINX(34, 232, 235)
    PINX(35, 236, 239) PINX(36, 240, 243) PINX(37, 244, 247) PINX(38, 248, 251) PINX(39, 252, 255)
  }
}
DEV bf16x8 split3(float v, bool side) {
  const uint32_t h = __builtin_bit_cast(uint32_t, v);
  const float r1 = v - bhi(h);
  const uint32_t m = __builtin_bit_cast(uint32_t, r1);
  const uint32_t l = __builtin_bit_cast(uint32_t, r1 - bhi(m));
  u32x4 o;
#define PK(LO, HI) __builtin_amdgcn_perm(HI, LO, 0x07060302)
  if (side) { o[0] = PK(h, h); o[1] = PK(m, h); o[2] = PK(l, m); o[3] = PK(m, l); }
  else { o[0] = PK(h, m); o[1] = PK(h, l); o[2] = PK(h, m); o[3] = PK(l, m); }
  return asbf(o);
}
DEV float sum4(f32x4 v) { return (v[0] + v[1]) + (v[2] + v[3]); }
DEV uint32_t nxt(uint32_t s) { return s + SLOT == RING ? 0u : s + SLOT; }
constexpr uint32_t SJA = 4 * SLOT + 16384, RSV = 16384;
constexpr uint32_t WS_CSG = 917504, WS_STG = 997376, OOB = 0x7ffffff0u;
template <int CTRL> DEV float dpp_add(float v) {
  return v + __builtin_bit_cast(float, __builtin_amdgcn_update_dpp(0, __builtin_bit_cast(int, v), CTRL, 0xf, 0xf, true));
}
DEV float wave_sum63(float v) {
  v = dpp_add<0x111>(v); v = dpp_add<0x112>(v); v = dpp_add<0x114>(v); v = dpp_add<0x118>(v);
  v += __builtin_bit_cast(float, __builtin_amdgcn_update_dpp(0, __builtin_bit_cast(int, v), 0x142, 0xa, 0xf, true));
  v += __builtin_bit_cast(float, __builtin_amdgcn_update_dpp(0, __builtin_bit_cast(int, v), 0x143, 0xc, 0xf, true));
  return v;
}
DEV void gran_fix(u32x2& g, char* ws, uint32_t off, uint32_t t32) {
  for (uint32_t it = 0; it < SPIN_CAP && !__all(g[1] == t32); ++it) {
    const u64 v = flag_load(ws, off);
    g[0] = (uint32_t)v; g[1] = (uint32_t)(v >> 32);
  }
}

DEV uint32_t kvq_voff(int piece, int lane, int i, bool isq) {
  uint32_t p = piece * 1024 + lane * 16;
  uint32_t row = (p * 52429u) >> 24;
  uint32_t col = p - row * 320;
  bool hi = col >= 160;
  uint32_t c = hi ? col - 160 : col;
  return isq ? (row + (hi ? 64u : 0u)) * 3840 + 160 * i + c
             : row * 3840 + (hi ? 2560u : 1280u) + 160 * i + c;
}
DEV void h_store(f32x16& ag, f32x16& au, float cg, float cu, float mean, float rs, int tid, rsrc_t rws, uint32_t hs) {
  const bf16x8 mf = split3(-mean, false);
  ag = MFMA32(split3(cg, true), mf, ag);
  au = MFMA32(split3(cu, true), mf, au);
  const float r2 = rs * rs;
#pragma unroll
  for (int j = 0; j < 16; ++j) ag[j] = (ag[j] * r2) * au[j];
  const int l2 = tid & 63, r = l2 & 31, hh = l2 >> 5;
  const uint32_t hv = (r >> 4) * (68 * 1024) + (hh * 16 + (r & 15)) * 16;
  st128_sc1(pack8(ag, 0), rws, hv, hs);
  st128_sc1(pack8(ag, 1), rws, hv + 512, hs);
}

DEV void l2_warm(uint32_t voff, rsrc_t r, uint32_t soff) {
  asm volatile("s_mov_b32 m0, %0\n\ts_nop 0\n\tbuffer_load_dword %1, %2, %3 offen sc1 lds" :: "s"(MISC + 16384), "v"(voff), "s"(r), "s"(soff) : "memory");
}
DEV void acheck(u32x2 f, rsrc_t r, uint32_t voff, uint32_t soff, u64 lo) {
  u64 m; uint32_t n;
  asm volatile("s_mov_b32 %2, 0x10000\n\ts_branch 2f\n1:\n\tbuffer_load_dwordx2 %0, %3, %4, %5 offen sc1\n\ts_waitcnt vmcnt(0)\n2:\n\t"
               "v_cmp_gt_u64 vcc, %6, %0\n\tv_cmp_lt_u64 %1, %7, %0\n\ts_sub_u32 %2, %2, 1\n\ts_cselect_b64 %1, 0, %1\n\ts_cselect_b64 vcc, 0, vcc\n\t"
               "s_or_b64 %1, %1, vcc\n\ts_cbranch_scc1 1b"
               : "+v"(f), "=&s"(m), "=&s"(n) : "v"(voff), "s"(r), "s"(soff), "s"(lo), "s"(lo | 0xff) : "memory", "vcc", "scc");
}
template <int MODE>
DEV void first_chunk(u32x4 (&xf)[40], float& mean, float& rs, u64& tag, int tid, int w, int b, int rg, int ci, rsrc_t rx,
                     rsrc_t rq, rsrc_t rg_, rsrc_t ru_, const void* gwd, rsrc_t rws, char* ws) {
  constexpr int D = MODE == 1 ? 5 : 4;
  const int l = tid & 63, hh = l >> 5, r = l & 31, rb = w & 3, nh = w >> 2;
  const uint32_t xvo = (8 * w + (l >> 3)) * 1280 + (((l & 7) ^ ((4 * w + (l >> 4)) & 7)) << 4);
  const uint32_t xso = rg * 128 * 1280;
  const uint32_t wk = w * 1024;
#define ISS_X(S, H) dma16(((2 * (S)) & 7) * SLOT + (H) * 8192 + wk, xvo, rx, xso + (S) * 128 + (H) * 64 * 1280)
#define ISS_W(S, H) dma16(((2 * (S) + 1) & 7) * SLOT + (H) * 8192 + wk, (H) == 0 ? v0 : (H) == 1 ? v1 : v2, MODE ? rq : (H) == 0 ? rg_ : ru_, fso + (S) * FSTR)
#define ISS_WW(S) do { ISS_W(S, 0); ISS_W(S, 1); if (MODE == 1) ISS_W(S, 2); } while (0)
#define ISS_N(J, H) dma16(((4 + (J)) % 6) * SLOT + (H) * 8192 + wk, guvo, (H) ? ru_ : rg_, nso + (J) * 64 * 3072)
  ISS_X(0, 0); ISS_X(0, 1);
  const uint32_t guvo = (8 * w + (l >> 3)) * 3072 + (((l & 7) ^ (((l >> 4) & 1) << 2)) << 4);
  const uint32_t v0 = MODE ? kvq_voff(w, l, ci, false) : guvo, v1 = MODE ? kvq_voff(w + 8, l, ci, false) : guvo;
  const uint32_t v2 = kvq_voff(rb + 16, l, ci, false);
  const uint32_t fso = MODE ? 0 : ci * 128, nso = (MODE ? 16 + ci : 8 + ci) * 128;
  constexpr uint32_t FSTR = MODE ? 64 * 3840 : 64 * 3072;
  ISS_WW(0);
  u32x2 eld = aload64(rws, 0, WS_E);
  const int strip = b >> 1, tok = rg * 128 + ((MODE ? 8 : 0) + ci) * 8 + w;
  const uint32_t sstr = strip < 30 ? 3840 : 3072, scol = strip < 30 ? strip : strip < 54 ? strip - 30 : strip - 54;
  const rsrc_t rsj = strip < 30 ? rq : strip < 54 ? rg_ : ru_;
  const uint32_t sjv = b < 156 ? (l >> 3) * sstr + (l & 7) * 16 : OOB;
  u32x4 sj[5], sx0 = aload128p(rx, l * 16, tok * 1280), sx1 = aload128p(rx, l < 16 ? l * 16 + 1024 : OOB, tok * 1280);
#pragma unroll
  for (int i = 0; i < 5; ++i) sj[i] = aload128p(rsj, sjv, ((b & 1) * 320 + w * 40 + i * 8) * sstr + scol * 128);
  ISS_X(1, 0); ISS_X(1, 1); ISS_WW(1);
  const int tb = b < 240 ? b : b - 240, t0ks = tb / 5, t0cg = tb - 5 * t0ks;
  const uint32_t xra = (32 * rb + r) * 128 + ((hh ^ ((r >> 1) & 7)) << 4);
  const uint32_t wa = MODE ? (8 * hh + ((l >> 2) & 3)) * 320 + ((l >> 4) & 1) * 32 + 8 * (l & 3) + (MODE - 1) * 192
                           : (8 * hh + ((l >> 2) & 3)) * 128 + 64 * (nh ^ ((l >> 3) & 1)) + 32 * ((l >> 4) & 1) + 8 * (l & 3);
  constexpr int KST = MODE ? 5120 : 2048, HO = MODE ? 1280 : 512, B1 = MODE ? 64 : 8192;
  const uint32_t CO0 = WS_CSG + (MODE == 0 ? 1920 + 64 * ci + 32 * nh : MODE == 1 ? 640 + 80 * ci : 1296 + 80 * ci) * 16;
  const uint32_t CO1 = CO0 + (MODE == 0 ? 1536 : 32) * 16;
  f32x16 a0 = {}, a1 = {}, a2 = {};
  bf16x8 w0, w1, w2;
  u32x4 gs; u32x2 g0, g1, g2;
  open_stage<D + 8>();
  lptr base = lp(SLOT + wa);
  xf[0] = lds_r128((lptr)xra);
  w0 = trd2(base, base + HO); w1 = trd2(base + B1, base + B1 + HO);
  if (MODE == 1) w2 = trd2(base + 128, base + 128 + HO);
#pragma unroll
  for (int t = 1; t < 4; ++t) xf[t] = lds_r128((lptr)(xra ^ (t << 5)));
#define MM(A, W) A = MODE ? MFMA32(asbf(xf[ks]), W, A) : MFMA32(W, asbf(xf[ks]), A)
#pragma unroll
  for (int s = 0; s < 10; ++s) {
#pragma unroll
    for (int t = 0; t < 4; ++t) {
      const int ks = 4 * s + t;
      lptr o = base + (t + 1) * KST;
      uint32_t xb = 0;
      SB0();
      pin<true>(ks, xf[ks]);
      MM(a0, w0);
      if (MODE == 1 && t == 3) MM(a1, w1);
      SB0();
      if (t == 3 && s < 9) {
        if (s == 0 || s == 2 || s == 3) open_stage<2 * D + 2>();
        else if (s == 1) open_stage<2 * D + 3>();
        else if (s == 6) open_stage<2 * D>();
        else if (s == 7) open_stage<D + 4>();
        else if (s == 8) open_stage<D + 3>();
        else open_stage<2 * D + 1>();
        o = base = lp(((2 * s + 3) & 7) * SLOT + wa);
        xb = opq(((2 * s + 2) & 7) * SLOT + xra);
        xf[ks + 1] = lds_r128((lptr)xb);
      }
      if (ks < 39) w0 = trd2(o, o + HO);
      if (s == 0 && t < 2) { ISS_X(2 + t, 0); ISS_X(2 + t, 1); }
      if (s >= 1 && s <= 6) { if (t == 0) ISS_X(s + 3, 0); if (t == 1) ISS_W(s + 3, 0); if (t == 2 && MODE == 1) ISS_W(s + 3, 2); }
      if ((s == 7 || s == 9) && t < 2) ISS_N(s - 7 + t, 0);
      SB0();
      if (MODE != 1 || t != 3) MM(a1, w1);
      SB0();
      if (ks < 39) w1 = trd2(o + B1, o + B1 + HO);
      if (s == 0 && t < 2) ISS_WW(2 + t);
      if (s >= 1 && s <= 6) { if (t == 0) ISS_X(s + 3, 1); if (t == 1) ISS_W(s + 3, 1); }
      if ((s == 7 || s == 9) && t < 2) ISS_N(s - 7 + t, 1);
      if (MODE == 1) { SB0(); MM(a2, w2); SB0(); if (ks < 39) w2 = trd2(o + 128, o + 128 + HO); }
      if (t == 3 && s < 9) {
#pragma unroll
        for (int u = 1; u < 4; ++u) xf[ks + 1 + u] = lds_r128((lptr)(xb ^ (u << 5)));
      }
      if (s == 0 && t == 1)
        dma16(nh * 2 * SLOT + 16384 + rb * 1024, (4 * w + (l >> 4)) * 1280 + (l & 15) * 16, mk_rsrc(gwd, 1536 * 1280), t0ks * 32 * 1280 + t0cg * 256);
      if (s == 0 && t == 2) {
        wait_vm<3 * D + 1>();
        fence_reg(eld); fence_reg(sx0); fence_reg(sx1);
        const u64 e = ((u64)(uint32_t)__builtin_amdgcn_readfirstlane(eld[1]) << 32) | (uint32_t)__builtin_amdgcn_readfirstlane(eld[0]);
        tag = (e * 6364136223846793005ull + 1442695040888963407ull);
        u32x4 idn;
#pragma unroll
        for (int q = 0; q < 4; ++q) idn[q] = ((l & 7) >> 1) == q ? ((l & 1) ? 0x3f800000u : 0x3f80u) : 0u;
        f32x4 d = {};
#pragma unroll
        for (int i = 0; i < 5; ++i) { fence_reg(sj[i]); d = MFMA16(asbf(idn), asbf(sj[i]), d); }
        float sm = 0.f, sq = 0.f;
#pragma unroll
        for (int q = 0; q < 8; ++q) {
          const uint32_t u = q < 4 ? sx0[q & 3] : sx1[q & 3];
          const float lo = blo(u), hi = bhi(u);
          sm += lo + hi; sq = fmaf(lo, lo, fmaf(hi, hi, sq));
        }
        lds_w128((lptr)(SJA + w * 512 + (l & 15) * 32 + ((l >> 4) & 1) * 16), __builtin_bit_cast(u32x4, d));
        sm = wave_sum63(sm) * (1.f / 640.f); sq = wave_sum63(sq);
        const float ri = __builtin_amdgcn_rsqf(fmaxf(sq * (1.f / 640.f) - sm * sm, 0.f) + 1e-5f);
        const uint32_t t32 = tag >> 32;
        const u32x4 og = {__builtin_bit_cast(uint32_t, sm), t32, __builtin_bit_cast(uint32_t, ri), t32};
        st128_sc1(og, rws, l == 63 ? 0u : OOB, WS_STG + tok * 16);
      }
      if (s == 1 && t == 2) {
        const lptr a = lp(SJA + l * 4);
        float v = 0.f;
#pragma unroll
        for (int j = 0; j < 16; ++j) v += lds_rf(a + j * 256);
        const u32x2 og = {__builtin_bit_cast(uint32_t, v), (uint32_t)(tag >> 32)};
        __builtin_amdgcn_raw_buffer_store_b64(og, rws, (w == 7 && b < 156) ? l * 16 : OOB, WS_CSG + strip * 1024 + (b & 1) * 8, 16);
      }
      if (s == 3 && t == 2) {
        const int g = l >> 4;
        const lptr a = lp((g >> 1) * 2 * SLOT + 16384 + (4 * (g & 1) + ((l >> 2) & 3)) * 256 + w * 32 + 8 * (l & 3));
        const bf16x8 f = trd2(a, a + 8 * 256);
        const int cb = 8 * t0cg + w, c5 = cb / 5, n5 = cb - 5 * c5;
        st128_sc1(__builtin_bit_cast(u32x4, f), rws, l * 16, WS_W2 + ((c5 * 68 + t0ks) * 5 + n5) * 1024);
      }
      if (MODE != 2 && s == 7 && t == 2) publish_if0(w, rws, WS_FT + b * 8, (tag & ~0xffull) | 1);
      if (s == 8 && t == 0) {
        const int l3 = opq(tid) & 63;
        gs = aload128(rws, (l3 & 31) * 16, WS_STG + (rg * 128 + rb * 32) * 16);
        const uint32_t cv = (l3 & 31) * 16 + (l3 >> 5) * 8;
        g0 = aload64(rws, cv, CO0); g1 = aload64(rws, cv, CO1);
        if (MODE == 1) g2 = aload64(rws, cv + ((l3 & 16) ? 560 * 16 : 0), CO0 + 1024);
      }
    }
  }
  SB0();
  const int l2 = opq(tid) & 63, r2 = l2 & 31;
  wait_vm<4>();
  fence_reg(gs); fence_reg(g0); fence_reg(g1); if (MODE == 1) fence_reg(g2);
  u32x2 sa = {gs[0], gs[1]}, sb = {gs[2], gs[3]};
  const uint32_t t32 = tag >> 32;
  if (__builtin_expect(!__all(sa[1] == t32 && sb[1] == t32 && g0[1] == t32 && g1[1] == t32 && (MODE != 1 || g2[1] == t32)), 0)) {
    const uint32_t so = WS_STG + (rg * 128 + rb * 32 + r2) * 16, cv = r2 * 16 + (l2 >> 5) * 8;
    gran_fix(sa, ws, so, t32); gran_fix(sb, ws, so + 8, t32); gran_fix(g0, ws, CO0 + cv, t32); gran_fix(g1, ws, CO1 + cv, t32);
    if (MODE == 1) gran_fix(g2, ws, CO0 + 1024 + cv + ((l2 & 16) ? 560 * 16 : 0), t32);
  }
  const uint32_t um = sa[0], ur = sb[0], u0 = g0[0], u1 = g1[0], u2 = MODE == 1 ? g2[0] : 0u;
  mean = __builtin_bit_cast(float, um); rs = __builtin_bit_cast(float, ur);
  const float c0 = __builtin_bit_cast(float, u0), c1 = __builtin_bit_cast(float, u1), c2 = __builtin_bit_cast(float, u2);
  if (MODE == 0) {
    h_store(a0, a1, c0, c1, mean, rs, l2, rws, WS_A2 + ((rg * 8 + 2 * rb) * 68 + 2 * ci + nh) * 1024);
  } else {
    lds_wf((lptr)(RSV + w * 128 + r2 * 4), rs);
    const bf16x8 mf = split3(-mean, false);
    a0 = MFMA32(mf, split3(c0, true), a0); a1 = MFMA32(mf, split3(c1, true), a1);
    if (MODE == 1) a2 = MFMA32(mf, split3(c2, true), a2);
    const lptr ra = lp(RSV + w * 128 + (l2 >> 5) * 16);
#pragma unroll
    for (int g = 0; g < 4; ++g) {
      const f32x4 rv = lds_rf4(ra + 32 * g);
#pragma unroll
      for (int j = 4 * g; j < 4 * g + 4; j += 2) {
        const f32x2 rr = {rv[j & 3], rv[(j & 3) + 1]};
        if (MODE == 1) {
          const f32x2 rl = rr * LOG2E, v = f32x2{a2[j], a2[j + 1]} * rr;
          PHI2(a0, j, rr, rl); PHI2(a1, j, rr, rl); PHI2(a2, j, rr, rl);
          if (l2 & 16) { a2[j] = v[0]; a2[j + 1] = v[1]; }
        } else {
          a0[j] *= rr[0]; a0[j + 1] *= rr[1]; a1[j] *= rr[0]; a1[j + 1] *= rr[1];
        }
      }
    }
    const lptr e0 = lp(MISC + (((MODE - 1) * 12 + rb) * 2) * 1024 + l2 * 16);
    lds_w128(e0, pack8(a0, 0)); lds_w128(e0 + 1024, pack8(a0, 1));
    lds_w128(e0 + 8192, pack8(a1, 0)); lds_w128(e0 + 8192 + 1024, pack8(a1, 1));
    if (MODE == 1) { lds_w128(e0 + 16384, pack8(a2, 0)); lds_w128(e0 + 16384 + 1024, pack8(a2, 1)); }
  }
}
constexpr int L2PF = 1;
constexpr int ORD = 0;
constexpr int PUBP = 1;
constexpr int KQ = 0;
constexpr uint32_t AOFF = KQ ? 69632 : 1024;
template <int NH>
DEV void q_chunk(u32x4 (&xf)[40], u32x4 (&A)[34], uint32_t as, uint32_t aw, float mean, float rs, int tid, int w, int ci, int rg, int ct,
                 uint32_t fb_row, rsrc_t rws, char* ws, u64 tag) {
  const int l = tid & 63, rb = w & 3;
  const uint32_t ta = (8 * (l >> 5) + ((l >> 2) & 3)) * 320 + ((l >> 4) & 1) * 32 + 8 * (l & 3) + NH * 128;
  const uint32_t ZS = MISC + 5120, X3 = MISC + 5632;
  const uint32_t l16 = l * 16;
  uint32_t cur = 2 * SLOT, prv = SLOT;
  f32x16 a0 = {}, a1 = {}; f32x4 pp; u32x2 f2;
  bf16x8 w0[2], w1[2];
  u32x2 g0, g1, fl; u32x4 z4; uint32_t z1;
  const uint32_t cv = (NH ? (l & 15) : (l & 31)) * 16 + (l >> 5) * 8, CO0 = WS_CSG + (80 * ci + 64 * NH) * 16;
  bar();
  lptr base = lp(cur + ta);
  w0[0] = trd2(base, base + 1280);
  if (NH == 0) w1[0] = trd2(base + 64, base + 64 + 1280);
  w0[1] = trd2(base + 5120, base + 5120 + 1280);
  if (NH == 0) w1[1] = trd2(base + 5120 + 64, base + 5120 + 64 + 1280);
#pragma unroll
  for (int s = 0; s < 5; ++s) {
#pragma unroll
    for (int t = 0; t < 8; ++t) {
      const int ks = 8 * s + t;
      SB0();
      pin<false>(ks, xf[ks]);
      a0 = MFMA32(w0[t & 1], asbf(xf[ks]), a0);
      SB0();
      if (t < 2 || (t == 2 && NH == 0))
        dma16_sc1(prv + (w + 8 * t) * 1024, l16, rws, WS_W2 + (ct * 340 + s * 20 + w + 8 * t) * 1024);
      if (NH == 0 && s == 2 && t == 0) publish_if0(w, rws, WS_FB + rg * 128 + ci * 8, (tag & ~0xffull) | 2);
      if (NH == 1 && s == 2 && t == 2) fl = aload64(rws, (l & 15) * 8, WS_FA + ci * 128);
      if (NH == 1 && s == 3 && t == 6) {
        wait_vm<11>(); acheck(fl, rws, (l & 15) * 8, WS_FA + ci * 128, (tag & ~0xffull) | 1);
        const uint32_t zv = (l & 15) * KVREC + (l >> 4) * 20, zo = WS_KVP + ci * 16 * KVREC + 6400 + rb * 80;
        z4 = aload128(rws, zv, zo); z1 = aload32(rws, zv, zo + 16);
      }
      if (s == 4 && t == 3) f2 = aload64(rws, (l & 7) * 8, fb_row);
      if (s == 3 && t == 3) { g0 = aload64(rws, cv, CO0); if (NH == 0) g1 = aload64(rws, cv, CO0 + 512); }
      if ((s == 2 || s == 3) && (t == 3 || t == 5)) {
        const int p = 2 * (s - 2) + (t == 5);
        A[2 * p] = aload128(rws, l16, as + p * 4096); A[2 * p + 1] = aload128(rws, l16, as + p * 4096 + AOFF);
      }
      if (t == 6 && s < 4) {
        if (s == 1) open_stage<NH ? 4 : 6>(); else bar();
        prv = cur; cur = nxt(cur); base = lp(cur + ta);
      }
      const int tn = (t + 2) & 7;
      const lptr o = base + (tn & 3) * 5120 + (tn >> 2) * 160;
      if (ks < 38) w0[t & 1] = trd2(o, o + 1280);
      if (NH == 0) {
        SB0();
        a1 = MFMA32(w1[t & 1], asbf(xf[ks]), a1);
        SB0();
        if (ks < 38) w1[t & 1] = trd2(o + 64, o + 64 + 1280);
      }
    }
  }
  SB0();
  const int l2 = opq(tid) & 63, r = l2 & 31, hh = l2 >> 5;
  {
        wait_vm<NH ? 9 : 8>(); fence_reg(g0); if (NH == 0) fence_reg(g1);
    const uint32_t u0 = g0[0], u1 = NH ? 0u : g1[0];
    const bf16x8 mf = split3(-mean, false);
    a0 = MFMA32(split3(__builtin_bit_cast(float, u0), true), mf, a0);
    if (NH == 0) a1 = MFMA32(split3(__builtin_bit_cast(float, u1), true), mf, a1);
  }
  const f32x2 rr = {rs, rs}, rl = rr * LOG2E;
  const lptr zs = lp(ZS + 16 * hh), x3 = lp(X3 + (32 * rb + r) * 4);
  if (NH == 0) {
#pragma unroll
    for (int j = 0; j < 16; j += 2) { PHI2(a0, j, rr, rl); PHI2(a1, j, rr, rl); }
    wait_vm<0>(); acheck(f2, rws, (l2 & 7) * 8, fb_row, (tag & ~0xffull) | 2);
    A[8] = aload128(rws, l16, as + 4 * 4096); A[9] = aload128(rws, l16, as + 4 * 4096 + AOFF);
    if (L2PF & 1) l2_warm(l2 < 12 ? (w * 12 + l2) * 128 : OOB, rws, aw + 5 * 4096);
    bar();
    float gs[8];
#pragma unroll
    for (int g = 0; g < 8; ++g) {
      const f32x4 z = lds_rf4(zs + 32 * g);
      float acc = 0.f;
#pragma unroll
      for (int j = 0; j < 4; ++j) acc = fmaf(g < 4 ? a0[4 * g + j] : a1[4 * (g - 4) + j], z[j], acc);
      gs[g] = acc;
    }
    float p0 = gs[0] + gs[1] + (hh ? 0.f : gs[2]);
    float p1 = gs[3] + gs[4] + (hh ? gs[2] : 0.f);
    float p2 = gs[5] + gs[6] + (hh ? 0.f : gs[7]);
    if (hh) lds_wf(x3, gs[7]);
    p0 += __shfl_xor(p0, 32); p1 += __shfl_xor(p1, 32); p2 += __shfl_xor(p2, 32);
    pp = f32x4{p0, p1, p2, gs[7]};
  } else {
#pragma unroll
    for (int j = 0; j < 8; j += 2) PHI2(a0, j, rr, rl);
      {
        wait_vm<3>(); fence_reg(z4); fence_reg(z1);
        const lptr zw = lp((l & 15) == 15 ? ZS + rb * 80 + (l >> 4) * 20 : ZS + 320);
#pragma unroll
        for (int j = 0; j < 5; ++j) {
          const uint32_t u = j < 4 ? z4[j & 3] : z1;
          float v = __builtin_bit_cast(float, u);
          v = dpp_add<0x111>(v); v = dpp_add<0x112>(v); v = dpp_add<0x114>(v); v = dpp_add<0x118>(v);
          lds_wf(zw + 4 * j, v);
        }
      }
    wait_vm<0>(); acheck(f2, rws, (l2 & 7) * 8, fb_row, (tag & ~0xffull) | 2);
    A[8] = aload128(rws, l16, as + 4 * 4096); A[9] = aload128(rws, l16, as + 4 * 4096 + AOFF);
    if (L2PF & 1) l2_warm(l2 < 12 ? (w * 12 + l2) * 128 : OOB, rws, aw + 5 * 4096);
    bar();
    float p3 = 0.f;
#pragma unroll
    for (int g = 0; g < 2; ++g) {
      const f32x4 z = lds_rf4(zs + 256 + 32 * g);
#pragma unroll
      for (int j = 0; j < 4; ++j) p3 = fmaf(a0[4 * g + j], z[j], p3);
    }
    p3 += __shfl_xor(p3, 32);
    if (hh) lds_wf(x3 + 512, p3);
    pp = f32x4{p3, p3, p3, p3};
  }
  const uint32_t pv = (r >> 4) * (68 * 1024) + (hh * 16 + (r & 15)) * 16;
  const uint32_t qb = WS_A2 + ((rg * 8 + 2 * rb) * 68 + 48) * 1024 + ci * 5 * 512;
  bar();
  const float r3 = __builtin_amdgcn_rcpf(pp[3] + lds_rf(x3 + (NH ? 0 : 512)) + 1e-6f);
  if (NH == 0) {
    const float r0 = __builtin_amdgcn_rcpf(pp[0] + 1e-6f), r1 = __builtin_amdgcn_rcpf(pp[1] + 1e-6f), r2 = __builtin_amdgcn_rcpf(pp[2] + 1e-6f);
    const float sc[8] = {r0, r0, hh ? r1 : r0, r1, r1, r2, r2, hh ? r3 : r2};
#pragma unroll
    for (int j = 0; j < 16; ++j) { a0[j] = a0[j] * sc[j >> 2]; a1[j] = a1[j] * sc[4 + (j >> 2)]; }
    st128_sc1(pack8(a0, 0), rws, pv, qb); st128_sc1(pack8(a0, 1), rws, pv, qb + 512);
    st128_sc1(pack8(a1, 0), rws, pv, qb + 1024); st128_sc1(pack8(a1, 1), rws, pv, qb + 1536);
  } else {
#pragma unroll
    for (int j = 0; j < 8; ++j) a0[j] = a0[j] * r3;
    st128_sc1(pack8(a0, 0), rws, pv, qb + 2048);
  }
}

__global__ __launch_bounds__(512, 2) void fused_kernel(const void* __restrict__ gx, const void* __restrict__ gwqkv,
                                                       const void* __restrict__ gwo, const void* __restrict__ gwg,
                                                       const void* __restrict__ gwu, const void* __restrict__ gwd,
                                                       void* __restrict__ gout, char* __restrict__ ws) {
  const int tid = threadIdx.x;
  const int w = __builtin_amdgcn_readfirstlane(tid >> 6);
  const int rb = w & 3, nh = w >> 2;
  const int b = blockIdx.x;
  const int xcd = b & 7, idx = b >> 3;
  const int rg = (xcd >> 2) * 8 + (idx >> 2);
  const bool iskv = (idx & 2) != 0;
  const int ci = 2 * (xcd & 3) + (idx & 1);
  const int tt = (xcd >> 1) * 8 + (idx >> 2), ct = (xcd & 1) * 4 + (idx & 3);

  const rsrc_t rx = mk_rsrc(gx, 2048 * 1280), rqkv = mk_rsrc(gwqkv, 640 * 3840);
  const rsrc_t rg_ = mk_rsrc(gwg, 640 * 3072), ru_ = mk_rsrc(gwu, 640 * 3072);
  const rsrc_t rws = mk_rsrc(ws, WS_USED);

  {
    u64 pc; asm volatile("s_getpc_b64 %0" : "=s"(pc));
    const uint32_t l = tid & 63;
    asm volatile("s_mov_b32 m0, %0\n\ts_nop 0\n\tbuffer_load_dword %1, %2, 0 offen lds"
                 :: "s"(6 * SLOT + 16384), "v"(l < 2 ? (idx * 16 + w * 2 + l) * 128 : OOB), "s"(mk_rsrc((const void*)pc, 64512)) : "memory");
  }
  u64 tag;
  u32x4 xf[40];
  float mean, rs;
  const uint32_t mode = ((idx >> 1) & 1) * (1 + nh);
  if (mode == 0) {
    first_chunk<0>(xf, mean, rs, tag, opq(tid), w, b, rg, ci, rx, rqkv, rg_, ru_, gwd, rws, ws);
  }
  if (opqs(mode) == 1) first_chunk<1>(xf, mean, rs, tag, opq(tid), w, b, rg, ci, rx, rqkv, rg_, ru_, gwd, rws, ws);
  if (opqs(mode) == 2) first_chunk<2>(xf, mean, rs, tag, opq(tid), w, b, rg, ci, rx, rqkv, rg_, ru_, gwd, rws, ws);
  if (iskv) {
    const int t2 = opq(tid), l = t2 & 63, hh = l >> 5, r = l & 31;
    bar();
    {
      const uint32_t guvo = (8 * w + (l >> 3)) * 3072 + (((l & 7) ^ (((l >> 4) & 1) << 2)) << 4), so = 4 * 64 * 3072 + (16 + ci) * 128;
      dma16(2 * SLOT + w * 1024, guvo, rg_, so); dma16(2 * SLOT + 8192 + w * 1024, guvo, ru_, so);
    }
    const int kb = w < 5 ? (w >> 1) : (w - 5), vb = 2 + ((w + 1) >> 1);
    const lptr ka = lp(MISC + kb * 8192 + l * 16), va = lp(MISC + vb * 8192 + l * 16);
    f32x16 d = {}, d2 = {};
    const uint32_t ck = 3 * SLOT;
    if (w < 5) {
#pragma unroll
      for (int j = 0; j < 8; j += 2) {
        d = MFMA32(asbf(lds_r128(ka + j * 1024)), asbf(lds_r128(va + j * 1024)), d);
        d2 = MFMA32(asbf(lds_r128(ka + j * 1024 + 1024)), asbf(lds_r128(va + j * 1024 + 1024)), d2);
      }
      const int ve = 32 * vb + r - 80;
      const int hv = (ve * 205) >> 12;
      const int e = ve - 20 * hv;
      const int lo = 20 * hv - 32 * kb - 4 * hh;
      const uint32_t ca = ck + (hv * 400 + e - lo * 20) * 4;
#pragma unroll
      for (int j = 0; j < 16; ++j) {
        const int rowb = (j & 3) + 8 * (j >> 2);
        lds_wf((lptr)((ve >= 0 && (unsigned)(rowb - lo) < 20u) ? ca : ck + 8192 + l * 4) + rowb * 80, d[j] + d2[j]);
      }
    } else {
      const u32x4 ones = {0x3f803f80u, 0x3f803f80u, 0x3f803f80u, 0x3f803f80u};
#pragma unroll
      for (int j = 0; j < 8; ++j) d = MFMA32(asbf(ones), asbf(lds_r128(ka + j * 1024)), d);
      if (hh == 0 && 32 * kb + r < 80) lds_wf((lptr)(ck + 6400 + (32 * kb + r) * 4), d[0]);
    }
    bar();
    st128_sc1(lds_r128((lptr)(ck + t2 * 16)), rws, t2 < 420 ? t2 * 16 : OOB, WS_KVP + (ci * 16 + rg) * KVREC);
  }

  const uint32_t fb_row = WS_FB + (tt >> 1) * 128;
  u32x2 pl; u32x4 wo4, k0, k1, k2, k3;
  {
    const int l = opq(tid) & 63;
    const uint32_t ga = (8 * (l >> 5) + ((l >> 2) & 3)) * 128 + 64 * (nh ^ ((l >> 3) & 1)) + 32 * ((l >> 4) & 1) + 8 * (l & 3);
    const uint32_t guvo = (8 * w + (l >> 3)) * 3072 + (((l & 7) ^ (((l >> 4) & 1) << 2)) << 4);
    const int c = iskv ? 16 + ci : 8 + ci;
    uint32_t nv0, nv1, nv2, nso, nstr; rsrc_t nr0;
    if (!iskv) { nv0 = kvq_voff(w, l, ci, true); nv1 = kvq_voff(w + 8, l, ci, true); nv2 = kvq_voff((w & 3) + 16, l, ci, true);
                 nr0 = rqkv; nso = 0; nstr = 128 * 3840; }
    else { nv0 = l * 16; nv1 = l * 16 + 8192; nv2 = l * 16 + 16384; nr0 = rws; nso = WS_W2 + (ct * 340 + w) * 1024; nstr = 20480; }
    nso = __builtin_amdgcn_readfirstlane(nso); nstr = __builtin_amdgcn_readfirstlane(nstr);
    const uint32_t third = nh ^ 1, wk = w * 1024;
    const uint32_t gso = opqs(c * 128);
    f32x16 ag = {}, au = {};
    bf16x8 w0[2], w1[2];
    u32x2 g0, g1, fl;
    const u64 tg1 = (tag & ~0xffull) | 1;
    const uint32_t isq = (~idx >> 1) & 1, fao = iskv ? WS_FA + ci * 128 : fb_row, fav = (l & (iskv ? 15 : 7)) * 8;
    bar();
    lptr base = lp(4 * SLOT + ga);
    w0[0] = trd2(base, base + 512); w1[0] = trd2(base + 8192, base + 8192 + 512);
    w0[1] = trd2(base + 2048, base + 2560); w1[1] = trd2(base + 10240, base + 10240 + 512);
#pragma unroll
    for (int s = 0; s < 10; ++s) {
#pragma unroll
      for (int t = 0; t < 4; ++t) {
        const int ks = 4 * s + t;
        const uint32_t prv = ((3 + s) % 6) * SLOT + wk;
        lptr o = base + ((t + 2) & 3) * 2048;
        SB0();
        pin<true>(ks, xf[ks]);
        ag = MFMA32(w0[t & 1], asbf(xf[ks]), ag);
        SB0();
        if (t == 2 && s < 9) {
          if (s == 0) bar(); else if (s == 1) open_stage<9>(); else if (s == 2) open_stage<10>();
          else if (s == 3) open_stage2<10, 9>(isq); else if (s == 8) open_stage<8>(); else open_stage<8>();
          o = base = lp(((5 + s) % 6) * SLOT + ga);
        }
        if (ks < 38) w0[t & 1] = trd2(o, o + 512);
        if (s == 0 && t == 0) dma16_if(isq, 2 * SLOT + wk, guvo, rg_, 4 * 64 * 3072 + gso);
        if (s < 5) {
          if (t == (s ? 0 : 1)) dma16(prv, guvo, rg_, (s + 5) * 64 * 3072 + gso);
          if (t == 1) dma16(prv + 8192, guvo, ru_, (s + 5) * 64 * 3072 + gso);
        } else {
          if (t == 0) dma16_sc1(prv, nv0, nr0, nso);
          if (t == 1) dma16_sc1(prv + 8192, nv1, nr0, nso);
        }
        SB0();
        au = MFMA32(w1[t & 1], asbf(xf[ks]), au);
        SB0();
        if (ks < 38) w1[t & 1] = trd2(o + 8192, o + 8192 + 512);
        if (s == 0 && t == 0) dma16_if(isq, 2 * SLOT + 8192 + wk, guvo, ru_, 4 * 64 * 3072 + gso);
        if (s >= 5 && t == 1) { dma16_if(third, prv + 16384, nv2, nr0, nso); nso += nstr; }
        if (s == 2 && t == 0) fl = aload64(rws, (l & 31) * 8, WS_FT + w * 256);
        if (s == 4 && t == 2) publish_if0(w, rws, iskv ? WS_FA + (ci * 16 + rg) * 8 : WS_FB + rg * 128 + ci * 8, tg1);
        if (s == 4 && t == 1) { wait_vm<5>(); acheck(fl, rws, (l & 31) * 8, WS_FT + w * 256, tg1); }
        if (s == 4 && t == 3) {
          const int n = w * 10 + l;
          l2_warm(l < 10 && iskv ? (n >> 2) * 1280 + min((n & 3) * 128, 316) : OOB, mk_rsrc(gwo, 640 * 1280), (4 * ci + (rg & 3)) * 20 * 1280 + (rg >> 2) * 320);
        }
        if (s == 5 && t == 3) {
          const uint32_t ln = ((idx >> 2) * 8 + w) * 18 + l;
          l2_warm(l < 18 && ln < 1120 ? ln * 128 : OOB, rws, WS_W2 + (ct * 340 + 100) * 1024);
        }
        if (s == 5 && t == 2) publish_if0(b | w, rws, WS_E, tag);
        if (s == 7 && t == 2) {
          const uint32_t cv = (l & 31) * 16 + (l >> 5) * 8, co = WS_CSG + (1920 + 64 * c + 32 * nh) * 16;
          g0 = aload64(rws, cv, co); g1 = aload64(rws, cv, co + 1536 * 16);
        }
        if (s == 7 && t == 3) fl = aload64(rws, fav, fao);
        if (s == 9 && t == 2) {
          wait_vm2<6, 4>(nh); acheck(fl, rws, fav, fao, tg1);
          const int t3 = opq(tid), row = (t3 * 3277) >> 16, rq = (t3 * 656) >> 16;
          const bool on = t3 < 400;
          const uint32_t vo = on ? rq * 4 * KVREC + (t3 - 100 * rq) * 16 : OOB, so = WS_KVP + ci * 16 * KVREC + (rg & 3) * 1600;
          asm volatile("s_cmp_lg_u32 %15, 0\n\ts_cbranch_scc1 1f\n\tbuffer_load_dwordx4 %0, %6, %9, %11 offen sc1\n\t"
                       "buffer_load_dwordx4 %1, %6, %9, %12 offen sc1\n\tbuffer_load_dwordx4 %2, %6, %9, %16 offen sc1\n\t"
                       "buffer_load_dwordx4 %3, %6, %9, %17 offen sc1\n\tbuffer_load_dwordx4 %4, %7, %10, %13 offen\n\t"
                       "buffer_load_dwordx2 %5, %8, %9, %14 offen sc1\n1:"
                       : "=&v"(k0), "=&v"(k1), "=&v"(k2), "=&v"(k3), "=&v"(wo4), "=&v"(pl)
                       : "v"(vo), "v"(on ? row * 1280 + (t3 - 20 * row) * 16 : OOB), "v"((l & 7) * 8), "s"(rws), "s"(mk_rsrc(gwo, 640 * 1280)),
                         "s"(so), "s"(so + KVREC), "s"((4 * ci + (rg & 3)) * 20 * 1280 + (rg >> 2) * 320), "s"(fb_row), "s"(isq),
                         "s"(so + 2 * KVREC), "s"(so + 3 * KVREC) : "memory", "scc");
        }
      }
    }
    SB0();
    const int l2 = opq(tid) & 63;
    fence_reg(g0); fence_reg(g1);
    const uint32_t u0 = g0[0], u1 = g1[0];
    h_store(ag, au, __builtin_bit_cast(float, u0), __builtin_bit_cast(float, u1), mean, rs, l2, rws, WS_A2 + ((rg * 8 + 2 * rb) * 68 + 2 * c + nh) * 1024);
  }
  uint32_t cur = 2 * SLOT, prv = SLOT;

  u32x4 A[34];
  const uint32_t as = WS_A2 + (KQ ? (tt * 4 + 2 * (w & 1)) * 68 + (w >> 1) : (tt * 4 + rb) * 68 + 2 * nh) * 1024;
  const uint32_t aw = WS_A2 + (tt * 4 + (idx & 3)) * 68 * 1024;
  const uint32_t role = iskv ? 2 : nh;
  const uint32_t WO = MISC, RED = MISC + 6400;
  if (role == 1) q_chunk<1>(xf, A, as, aw, mean, rs, opq(tid), w, ci, rg, ct, fb_row, rws, ws, tag);
  else if (role == 0) q_chunk<0>(xf, A, as, aw, mean, rs, opq(tid), w, ci, rg, ct, fb_row, rws, ws, tag);
  else {
    const int t4 = opq(tid), l = t4 & 63;
    wait_vm<2>();
    fence_reg(wo4); fence_reg(k0); fence_reg(k1); fence_reg(k2); fence_reg(k3);
    if (t4 < 400) {
      lds_w128((lptr)(WO + t4 * 16), wo4);
      f32x4 sum = (__builtin_bit_cast(f32x4, k0) + __builtin_bit_cast(f32x4, k1)) + (__builtin_bit_cast(f32x4, k2) + __builtin_bit_cast(f32x4, k3));
      lds_w128((lptr)(RED + t4 * 16), __builtin_bit_cast(u32x4, sum));
    }
    acheck(pl, rws, (t4 & 7) * 8, fb_row, (tag & ~0xffull) | 2);
#pragma unroll
    for (int p = 0; p < 5; ++p) { A[2 * p] = aload128(rws, l * 16, as + p * 4096); A[2 * p + 1] = aload128(rws, l * 16, as + p * 4096 + AOFF); }
    if (L2PF & 1) l2_warm(l < 12 ? (w * 12 + l) * 128 : OOB, rws, aw + 5 * 4096);
    bar();
    const int hl = rg & 3, qd = rg >> 2, head = 4 * ci + hl;
    const int q4 = (l >> 2) & 3, p4 = l & 3;
    {
      const int i16 = l & 15, g = l >> 4;
      u32x4 af[2];
#pragma unroll
      for (int db = 0; db < 2; ++db) {
        const int d = min(16 * db + i16, 19);
        f32x4 lo = {}, hi = {};
        const lptr al = lp(RED + d * 80 + min(g, 2) * 32), ah = lp(RED + d * 80 + min(g, 2) * 32 + (g < 2 ? 16 : 0));
#pragma unroll
        for (int j = 0; j < 4; ++j) {
          lo += lds_rf4(al + j * 1600);
          hi += lds_rf4(ah + j * 1600);
        }
        af[db][0] = g < 3 ? pk2(lo[0], lo[1]) : 0u; af[db][1] = g < 3 ? pk2(lo[2], lo[3]) : 0u;
        af[db][2] = g < 2 ? pk2(hi[0], hi[1]) : 0u; af[db][3] = g < 2 ? pk2(hi[2], hi[3]) : 0u;
      }
      const uint32_t b0 = WO + min(8 * g + q4, 16 + q4) * 320 + 8 * p4, b1 = WO + min(8 * g + 4 + q4, 16 + q4) * 320 + 8 * p4;
      for (int n = w; n < 10; n += 8) {
        const bf16x8 bf = trd2((lptr)(b0 + n * 32), (lptr)(b1 + n * 32));
        const f32x4 z4 = {};
        const f32x4 m0 = MFMA16(asbf(af[0]), bf, z4), m1 = MFMA16(asbf(af[1]), bf, z4);
        const int cb = 10 * qd + n, c5 = cb / 5, n5 = cb - 5 * c5;
        const uint32_t fb = WS_W2 + (c5 * 340 + 240 + n5) * 1024 + i16 * 16;
        const int G0 = 5 * head + g, G1 = 5 * head + 4;
        const uint32_t o0 = fb + (G0 >> 3) * 5120 + (((G0 >> 2) & 1) * 2 + (G0 & 1)) * 256 + ((G0 >> 1) & 1) * 8;
        const uint32_t o1 = fb + (G1 >> 3) * 5120 + (((G1 >> 2) & 1) * 2 + (G1 & 1)) * 256 + ((G1 >> 1) & 1) * 8;
        u32x2 v0 = {pk2(m0[0], m0[1]), pk2(m0[2], m0[3])}, v1 = {pk2(m1[0], m1[1]), pk2(m1[2], m1[3])};
        __builtin_amdgcn_raw_buffer_store_b64(v0, rws, o0, 0, 16);
        if (g == 0) __builtin_amdgcn_raw_buffer_store_b64(v1, rws, o1, 0, 16);
      }
    }
  }
  const int t5 = opq(tid), l = t5 & 63;
  const int pr0 = (t5 * 6554) >> 16;
  const int pr1 = ((t5 + 512) * 6554) >> 16;
  const uint32_t xo0 = pr0 * 1280 + (t5 - 10 * pr0) * 16, xo1 = t5 < 128 ? pr1 * 1280 + (t5 + 512 - 10 * pr1) * 16 : OOB;
  const uint32_t xs = tt * 64 * 1280 + ct * 160;
  const uint32_t l16 = l * 16;
  if (!iskv) { cur = SLOT; prv = 0; }
  const uint32_t pub_off = iskv ? WS_FM + ((rg >> 2) * 32 + 4 * ci + (rg & 3)) * 8 : WS_FB + rg * 128 + ci * 8;

  {
    const int tid = t5;
    u32x4 xr0 = aload128p(rx, xo0, xs), xr1 = aload128p(rx, xo1, xs);
    const uint32_t po2 = fb_row + 64 + (l & 7) * 8;
    const uint32_t po3 = l < 32 ? WS_FM + (ct >> 1) * 256 + l * 8 : fb_row + (l & 7) * 8;
    const u64 tg3 = (tag & ~0xffull) | 3;
    f32x4 acc[10] = {};
    u32x4 B[2][10];
#pragma unroll
    for (int p = 0; p < 17; ++p) {
      const int lo = p < 5 ? 0 : p - 4, hi = p - 1 < 11 ? p - 1 : 11;
      const int nop = hi >= lo ? hi - lo + 1 : 0, ex = p < PUBP ? 2 * (4 - p) + 3 : p == PUBP ? 2 : 0;
      if (p == 4) { wait_vm<8>(); acheck(pl, rws, po2, 0, tg3); if (L2PF & 2) l2_warm(l < 12 ? (w * 12 + l) * 128 : OOB, rws, aw + 9 * 4096); }
      if (p == 7) { wait_vm<10>(); acheck(pl, rws, po3, 0, tg3);
        if (L2PF & 4) l2_warm(l < 16 ? aw + 13 * 4096 + (w * 16 + l) * 128 : l < 26 ? WS_W2 + (ct * 340 + 260) * 1024 + (((idx >> 2) * 8 + w) * 10 + l - 16) * 128 : OOB, rws, 0);
      }
      switch (nop * 16 + ex) {
#define OPEN_CASE(NOP, EX) case NOP * 16 + EX: if (p < 12) open_stage<NOP * 5 + EX>(); else open_nl<NOP * 5 + EX>(); break;
        OPEN_CASE(0, 11) OPEN_CASE(1, 9) OPEN_CASE(2, 2) OPEN_CASE(1, 2) OPEN_CASE(4, 0)
        OPEN_CASE(3, 0) OPEN_CASE(2, 0) OPEN_CASE(1, 0) OPEN_CASE(0, 0)
        default: open_stage<0>();
      }
      fence_reg(A[2 * p]); fence_reg(A[2 * p + 1]);
      if (p == 0) publish_if0((~idx & 2) | w, rws, WS_FB + rg * 128 + (8 + ci) * 8, tg3);
      if (p == PUBP) publish_if0(w, rws, pub_off, tg3);
      if (p == 2 || p == 5) pl = aload64(rws, p == 2 ? po2 : po3, 0);
      if (p == 4) { A[16] = aload128(rws, l16, as + 8 * 4096); A[17] = aload128(rws, l16, as + 8 * 4096 + AOFF); }
      const lptr rbs = lp(cur + (KQ ? (w >> 1) * 5120 : nh * 10240) + l16);
#define PM(J) if (p > 0) { PM1(2 * J); PM1(2 * J + 1); } SB0()
#define PM1(J) acc[KQ ? (J) : (J) % 5] = MFMA16(asbf(B[q ^ 1][KQ ? (J) / 2 : (J)]), asbf(A[2 * p - 2 + (KQ ? (J) & 1 : (J) / 5)]), acc[KQ ? (J) : (J) % 5])
      const int q = p & 1;
      const bool ld = p + 5 < 17;
      const uint32_t so = WS_W2 + (ct * 340 + (p + 5) * 20) * 1024;
      if (ld) { dma16_sc1(prv + w * 1024, l16, rws, so + w * 1024); if (ORD) dma16_sc1(prv + (w + 8) * 1024, l16, rws, so + (w + 8) * 1024); }
      SB0();
#pragma unroll
      for (int j = 0; j < (KQ ? 5 : 10); ++j) B[q][j] = lds_r128(rbs + j * 1024);
      SB0();
      PM(0);
      if (ld && !ORD) dma16_sc1(prv + (w + 8) * 1024, l16, rws, so + (w + 8) * 1024);
      SB0(); PM(1);
      if (ld) dma8_sc1(prv + 16384 + w * 512, l16, rws, so + 16384 + w * 512);
      SB0(); PM(2); PM(3);
      if (ld && p != 3) A[2 * p + 10] = aload128(rws, l16, as + (p + 5) * 4096);
      SB0(); PM(4);
      if (ld && p != 3) A[2 * p + 11] = aload128(rws, l16, as + (p + 5) * 4096 + AOFF);
      SB0();
      prv = cur; cur = nxt(cur);
    }
#pragma unroll
    for (int j = 0; j < 10; ++j) { const int p = 17, q = 1; PM1(j); }
    const uint32_t fb = iskv ? SLOT : 0;
    if (KQ) bar();
    const lptr ow = lp(fb + (w * (KQ ? 32 : 16) + (l & 15)) * 336 + (l >> 4) * 16);
#pragma unroll
    for (int j = 0; j < (KQ ? 10 : 5); ++j) lds_w128(ow + (KQ ? (j >> 1) * 64 + (j & 1) * 16 * 336 : j * 64), __builtin_bit_cast(u32x4, acc[j]));
    bar();
    const rsrc_t ro = mk_rsrc(gout, 2048 * 1280);
#pragma unroll
    for (int h = 0; h < 2; ++h) {
      if (h == 0 || tid < 128) {
        u32x4 xr = h ? xr1 : xr0;
        fence_reg(xr);
        const int pr = h ? pr1 : pr0, pc = tid + 512 * h - 10 * pr;
        const lptr a = lp(fb + pr * 336 + pc * 32);
        f32x4 u0 = (lds_rf4(a) + lds_rf4(a + 64 * 336));
        f32x4 u1 = (lds_rf4(a + 16) + lds_rf4(a + 16 + 64 * 336));
        if (KQ) { u0 += lds_rf4(a + 128 * 336) + lds_rf4(a + 192 * 336); u1 += lds_rf4(a + 16 + 128 * 336) + lds_rf4(a + 16 + 192 * 336); }
        u32x4 o;
        o[0] = pk2(u0[0] + blo(xr[0]), u0[1] + bhi(xr[0])); o[1] = pk2(u0[2] + blo(xr[1]), u0[3] + bhi(xr[1]));
        o[2] = pk2(u1[0] + blo(xr[2]), u1[1] + bhi(xr[2])); o[3] = pk2(u1[2] + blo(xr[3]), u1[3] + bhi(xr[3]));
        asm volatile("buffer_store_dwordx4 %0, %1, %2, %3 offen\n\ts_nop 1" :: "v"(o), "v"(h ? xo1 : xo0), "s"(ro), "s"(xs) : "memory");
      }
    }
  }
}

extern "C" void kernel_launch(void* const* d_in, const int* in_sizes, int n_in, void* d_out, int out_size, void* d_ws,
                              size_t ws_size, hipStream_t stream) {
  (void)in_sizes; (void)n_in; (void)out_size; (void)ws_size;
  (void)hipFuncSetAttribute((const void*)fused_kernel, hipFuncAttributeMaxDynamicSharedMemorySize, LDS_BYTES);
  fused_kernel<<<256, 512, LDS_BYTES, stream>>>(d_in[0], d_in[1], d_in[2], d_in[4], d_in[5], d_in[6], d_out, (char*)d_ws);
}
```
